```python
import torch

def _softmax(x, axis=-1):
    return torch.softmax(x, dim=axis)
def _gelu(x):
    return torch.nn.functional.gelu(x, approximate="tanh")
def _silu(x):
    return torch.nn.functional.silu(x)
def _sigmoid(x):
    return torch.sigmoid(x)
def _layernorm(x, axis=-1, eps=1e-5):
    m = x.mean(dim=axis, keepdim=True)
    v = x.var(dim=axis, keepdim=True, unbiased=False)
    return (x - m) / torch.sqrt(v + eps)
def _scatter_add(tgt, idx, src, axis=0):
    return tgt.clone().index_add_(axis, idx, src)
def _segment_sum(src, seg, n):
    out = torch.zeros((n,) + src.shape[1:], dtype=src.dtype)
    return out.index_add_(0, seg, src)
def _pad(x, pw):
    flat = tuple(v for pair in reversed(pw) for v in pair)
    return torch.nn.functional.pad(x, flat)

kernel_name = 'sequential_preLN__ssm_conv_s4d__dense_activation_relu__prompt'

def setup_inputs():

    _w = (lambda a: (lambda t: t.to(torch.bfloat16) if t.is_floating_point() else t)(torch.as_tensor(a)))
    def _A_setup():
        Bsz, L, D, N = 1, 2048, 512, 128
        return {
            "u": _w((lambda *s: torch.randn(*s, dtype=torch.bfloat16) * 0.05)(Bsz, L, D)),
            "log_A_real": _w((lambda *s: torch.randn(*s, dtype=torch.bfloat16) * 6.4)(D, N) * 0.5),
            "A_imag": _w((lambda *s: torch.randn(*s, dtype=torch.bfloat16) * 0.05)(D, N) * 3.14),
            "C_real": _w((lambda *s: torch.randn(*s, dtype=torch.bfloat16) * 0.05)(D, N) / N ** 0.5),
            "C_imag": _w((lambda *s: torch.randn(*s, dtype=torch.bfloat16) * 0.05)(D, N) / N ** 0.5),
            "log_dt": _w((lambda *s: torch.randn(*s, dtype=torch.bfloat16) * 0.05)(D) * 0.5 - 4.0),
            "D_skip": _w((lambda *s: torch.randn(*s, dtype=torch.bfloat16) * 0.05)(D)),
        }
    _A_in = _A_setup()

    def _B_setup():
        B, S, D, Dff = 1, 2048, 512, 1792
        return {
            "x": _w((lambda *s: torch.randn(*s, dtype=torch.bfloat16) * 0.05)(B, S, D)),
            "w1": _w((lambda *s: torch.randn(*s, dtype=torch.bfloat16) * 0.05)(D, Dff)),
            "w2": _w((lambda *s: torch.randn(*s, dtype=torch.bfloat16) * 0.05)(Dff, D)),
        }
    _B_in = _B_setup()

    return {
        "A_u": _A_in["u"],
        "A_log_A_real": _A_in["log_A_real"],
        "A_A_imag": _A_in["A_imag"],
        "A_C_real": _A_in["C_real"],
        "A_C_imag": _A_in["C_imag"],
        "A_log_dt": _A_in["log_dt"],
        "A_D_skip": _A_in["D_skip"],
        "B_x": _B_in["x"],
        "B_w1": _B_in["w1"],
        "B_w2": _B_in["w2"],
    }

def reference(A_u, A_log_A_real, A_A_imag, A_C_real, A_C_imag, A_log_dt, A_D_skip, B_x, B_w1, B_w2):
    def _A_ref(u, log_A_real, A_imag, C_real, C_imag, log_dt, D_skip):
        L = u.shape[1]
        dt = torch.exp(log_dt)
        A = -torch.exp(log_A_real) + 1j * A_imag
        C = (C_real + 1j * C_imag) * dt[:, None]
        pos = (lambda x, dt: x.to(dt))(torch.arange(L), log_dt.dtype)
        P = torch.exp((A * dt[:, None])[:, :, None] * pos[None, None, :])
        K = 2.0 * torch.einsum("dn,dnl->dl", C, P).real
        L2 = 2 * L
        u_t = (lambda x, *d: x.permute(*d))(u, 0, 2, 1)
        Kf = (lambda x, *a, **k: torch.fft.rfft(x.float(), *a, **k))(K, L2)
        Uf = (lambda x, *a, **k: torch.fft.rfft(x.float(), *a, **k))(u_t, L2)
        y = (lambda x, *a, **k: torch.fft.irfft(x, *a, **k).to(torch.bfloat16))(Uf * Kf[None, :, :], L2)[:, :, :L]
        y = (lambda x, *d: x.permute(*d))(y, 0, 2, 1)
        return y + u * D_skip[None, None, :]

    def _B_ref(x, w1, w2):
        h = torch.einsum("bsd,df->bsf", x, w1)
        zero = torch.zeros_like(h)
        h = torch.maximum(h, zero)
        return torch.einsum("bsf,fd->bsd", h, w2)

    x = A_u
    h = x + _A_ref(_layernorm(x, axis=-1), A_log_A_real, A_A_imag, A_C_real, A_C_imag, A_log_dt, A_D_skip)
    h2 = h + _B_ref(_layernorm(h, axis=-1), B_w1, B_w2)
    return h2
```

```cpp
#include <hip/hip_runtime.h>
#include <hip/hip_bf16.h>
#include <stdint.h>

typedef unsigned short u16;
__device__ __forceinline__ float bf2f(u16 v) { return __uint_as_float(((unsigned)v) << 16); }
__device__ __forceinline__ u16 f2bf(float f) {
    unsigned u = __float_as_uint(f);
    return (u16)((u + 0x7FFF + ((u >> 16) & 1)) >> 16);
}
__device__ __forceinline__ float rbf(float f) { return bf2f(f2bf(f)); }

constexpr int L = 2048, D = 512, N = 128, F = 1792;

__global__ void k_ln(const u16* x, u16* u) {
    int t = blockIdx.x; int lane = threadIdx.x;
    float v[8]; float s = 0;
    for (int j = 0; j < 8; ++j) { v[j] = bf2f(x[t * D + lane * 8 + j]); s += v[j]; }
    for (int o = 32; o; o >>= 1) s += __shfl_xor(s, o);
    float m = s / D; float q = 0;
    for (int j = 0; j < 8; ++j) q += (v[j] - m) * (v[j] - m);
    for (int o = 32; o; o >>= 1) q += __shfl_xor(q, o);
    float mb = rbf(m); float var = rbf(q / D);
    float den = rbf(sqrtf(rbf(var + 1e-5f)));
    for (int j = 0; j < 8; ++j) u[t * D + lane * 8 + j] = f2bf(rbf(v[j] - mb) / den);
}

__global__ void k_kgen(const u16* logA, const u16* Aim, const u16* Cr, const u16* Ci, const u16* logdt, float* K) {
    int d = blockIdx.y; int l = blockIdx.x * 256 + threadIdx.x;
    float dt = rbf(expf(bf2f(logdt[d])));
    float pos = rbf((float)l);
    float acc = 0;
    for (int n = 0; n < N; ++n) {
        float zr = -rbf(expf(bf2f(logA[d * N + n]))) * dt;
        float zi = bf2f(Aim[d * N + n]) * dt;
        float cr = bf2f(Cr[d * N + n]) * dt, ci = bf2f(Ci[d * N + n]) * dt;
        float e = expf(zr * pos); float s, c; sincosf(zi * pos, &s, &c);
        acc += cr * e * c - ci * e * s;
    }
    K[d * L + l] = 2.f * acc;
}

__global__ void k_conv(const u16* x, const u16* u, const float* K, const u16* Dsk, u16* h) {
    int t = blockIdx.x; int d = threadIdx.x;
    float acc = 0;
    for (int s = 0; s <= t; ++s) acc += K[d * L + (t - s)] * bf2f(u[s * D + d]);
    float y = rbf(acc);
    float uu = bf2f(u[t * D + d]);
    float a = rbf(y + rbf(uu * bf2f(Dsk[d])));
    h[t * D + d] = f2bf(bf2f(x[t * D + d]) + a);
}

__global__ void k_g1(const u16* a, const u16* w1, u16* H) {
    int t = blockIdx.y; int f = blockIdx.x * 256 + threadIdx.x;
    float acc = 0;
    for (int k = 0; k < D; ++k) acc += bf2f(a[t * D + k]) * bf2f(w1[k * F + f]);
    H[t * F + f] = f2bf(fmaxf(rbf(acc), 0.f));
}
__global__ void k_g2(const u16* H, const u16* w2, const u16* h, u16* out) {
    int t = blockIdx.x; int d = threadIdx.x;
    float acc = 0;
    for (int k = 0; k < F; ++k) acc += bf2f(H[t * F + k]) * bf2f(w2[k * D + d]);
    out[t * D + d] = f2bf(bf2f(h[t * D + d]) + rbf(acc));
}

extern "C" void kernel_launch(void* const* d_in, const int* in_sizes, int n_in,
                              void* d_out, int out_size, void* d_ws, size_t ws_size,
                              hipStream_t stream) {
    const u16* x = (const u16*)d_in[0];
    char* ws = (char*)d_ws;
    u16* u = (u16*)(ws);
    float* K = (float*)(ws + (4 << 20));
    u16* h = (u16*)(ws + (8 << 20));
    u16* a2 = (u16*)(ws + (12 << 20));
    u16* H = (u16*)(ws + (16 << 20));
    k_ln<<<L, 64, 0, stream>>>(x, u);
    k_kgen<<<dim3(L / 256, D), 256, 0, stream>>>((const u16*)d_in[1], (const u16*)d_in[2], (const u16*)d_in[3], (const u16*)d_in[4], (const u16*)d_in[5], K);
    k_conv<<<L, D, 0, stream>>>(x, u, K, (const u16*)d_in[6], h);
    k_ln<<<L, 64, 0, stream>>>(h, a2);
    k_g1<<<dim3(F / 256, L), 256, 0, stream>>>(a2, (const u16*)d_in[8], H);
    k_g2<<<L, D, 0, stream>>>(H, (const u16*)d_in[9], h, (u16*)d_out);
}
```

```cpp
#include <hip/hip_runtime.h>
#include <stdint.h>

typedef unsigned short u16;
typedef unsigned u32;
typedef u32 u32x4 __attribute__((ext_vector_type(4)));
typedef u32 u32x2 __attribute__((ext_vector_type(2)));
typedef float f32x2 __attribute__((ext_vector_type(2)));
typedef float f32x4 __attribute__((ext_vector_type(4)));
typedef float f32x16 __attribute__((ext_vector_type(16)));
typedef __bf16 bf16x2 __attribute__((ext_vector_type(2)));
typedef __bf16 bf16x8 __attribute__((ext_vector_type(8)));
typedef _Float16 f16x2 __attribute__((ext_vector_type(2)));
typedef _Float16 f16x8 __attribute__((ext_vector_type(8)));
typedef short s16x4 __attribute__((ext_vector_type(4)));
#define LAS __attribute__((address_space(3)))

constexpr u32 WS_EPOCH = 0;
constexpr u32 WS_STATS = 4096;
constexpr u32 WS_FLAG1 = 40960;
constexpr u32 WS_FLAG2 = 77824;
constexpr u32 WS_H1    = 131072;
constexpr u32 WS_XT    = WS_H1 + (2u << 20);
constexpr u32 WS_HH    = WS_XT + (4u << 20);
constexpr u32 WS_STAMP = 200u << 20;

constexpr u32 L_TAB   = 0;
constexpr u32 TROW = 528, TCH = 53 * 528;
constexpr u32 L_KC    = 0;
constexpr u32 L_UC    = 33280;
constexpr u32 L_XTILE = 55968;
constexpr u32 L_T     = 56064;
constexpr u32 L_SC    = 64448;
constexpr u32 L_ST    = 64480;
constexpr u32 L_Y     = 0;
constexpr u32 L_SCR   = 155648;
constexpr u32 LDS_BYTES = 163840;

struct ExpTab { u16 v[256 * 16]; };
constexpr ExpTab make_exptab() {
    ExpTab t{};
    for (int mu = 0; mu < 256; ++mu)
        for (int k = 0; k < 16; ++k) {
            int l = 8 * mu + k - 3, off = 128;
            if (k < 11 && l >= 0) {
                int s = (l >= 1024) + (l >= 512) + (l >= 256);
                int pos = s ? (((l + (1 << (s - 1)) - 1 + ((l >> s) & 1)) >> s) << s) : l;
                int i = pos >> 8, col = (pos >> 3) & 31, bb = pos & 7;
                int row = i == 0 ? bb : i == 1 ? 8 + bb / 2 : i < 4 ? 12 + 2 * (i - 2) + bb / 4 : 12 + i;
                off = row * 144 + col * 4;
            }
            t.v[mu * 16 + k] = (u16)off;
        }
    return t;
}
static __device__ const ExpTab g_exptab = make_exptab();

#define BAR() asm volatile("s_waitcnt lgkmcnt(0)\n\ts_barrier" ::: "memory")
#define VMW(n) asm volatile("s_waitcnt vmcnt(%0)" :: "i"(n) : "memory")
#define STAMP(k)

__device__ __forceinline__ float bflo(u32 v) { return __uint_as_float(v << 16); }
__device__ __forceinline__ float bfhi(u32 v) { return __uint_as_float(v & 0xffff0000u); }
__device__ __forceinline__ u32 pkbf(float a, float b) { f32x2 x = {a, b}; bf16x2 r = __builtin_convertvector(x, bf16x2); return __builtin_bit_cast(u32, r); }
__device__ __forceinline__ u32 pkh(float a, float b) { f32x2 x = {a, b}; f16x2 r = __builtin_convertvector(x, f16x2); return __builtin_bit_cast(u32, r); }
__device__ __forceinline__ float rbf(float a) { return bflo(pkbf(a, a)); }
template <int CTRL> __device__ __forceinline__ float dpp_add(float v) {
    return v + __int_as_float(__builtin_amdgcn_update_dpp(0, __float_as_int(v), CTRL, 0xf, 0xf, true));
}
template <int CTRL> __device__ __forceinline__ float dpp_max(float v) {
    return fmaxf(v, __int_as_float(__builtin_amdgcn_update_dpp(0, __float_as_int(v), CTRL, 0xf, 0xf, true)));
}
__device__ __forceinline__ float wave_sum(float v) {
    v = dpp_add<0xb1>(v); v = dpp_add<0x4e>(v); v = dpp_add<0x141>(v); v = dpp_add<0x140>(v);
    int i = __float_as_int(v);
    return (__int_as_float(__builtin_amdgcn_readlane(i, 0)) + __int_as_float(__builtin_amdgcn_readlane(i, 16))) +
           (__int_as_float(__builtin_amdgcn_readlane(i, 32)) + __int_as_float(__builtin_amdgcn_readlane(i, 48)));
}
__device__ __forceinline__ float wave_max(float v) {
    v = dpp_max<0xb1>(v); v = dpp_max<0x4e>(v); v = dpp_max<0x141>(v); v = dpp_max<0x140>(v);
    int i = __float_as_int(v);
    return fmaxf(fmaxf(__int_as_float(__builtin_amdgcn_readlane(i, 0)), __int_as_float(__builtin_amdgcn_readlane(i, 16))),
                 fmaxf(__int_as_float(__builtin_amdgcn_readlane(i, 32)), __int_as_float(__builtin_amdgcn_readlane(i, 48))));
}
__device__ __forceinline__ void dma16(const void* sbase, u32 voff, u32 lds) {
    asm volatile("s_mov_b32 m0, %2\n\ts_nop 0\n\tglobal_load_lds_dwordx4 %0, %1" :: "v"(voff), "s"(sbase), "s"(lds) : "memory");
}
__device__ __forceinline__ void dma16_dev(const void* sbase, u32 voff, u32 lds) {
    asm volatile("s_mov_b32 m0, %2\n\ts_nop 0\n\tglobal_load_lds_dwordx4 %0, %1 sc1" :: "v"(voff), "s"(sbase), "s"(lds) : "memory");
}
struct cplx { float r, i; };
__device__ __forceinline__ cplx cmul(cplx a, cplx b) { return {a.r * b.r - a.i * b.i, a.r * b.i + a.i * b.r}; }
__device__ __forceinline__ cplx cexp_m(float zr, float zi, float m) {
    float mag = __builtin_amdgcn_exp2f(zr * m * 1.44269504088896341f);
    float p = zi * m;
    float k = __builtin_rintf(p * 0.15915494309189535f);
    float f = __builtin_fmaf(p, 0.15915494309189535f, -k) + p * 6.4206383e-9f;
    return {mag * __builtin_amdgcn_cosf(f), mag * __builtin_amdgcn_sinf(f)};
}
__device__ __forceinline__ bf16x8 trfrag(LAS char* p0, LAS char* p1) {
    s16x4 a = __builtin_amdgcn_ds_read_tr16_b64_v4i16((LAS s16x4*)p0);
    s16x4 b = __builtin_amdgcn_ds_read_tr16_b64_v4i16((LAS s16x4*)p1);
    u32x2 ua = __builtin_bit_cast(u32x2, a), ub = __builtin_bit_cast(u32x2, b);
    u32x4 r = {ua.x, ua.y, ub.x, ub.y};
    return __builtin_bit_cast(bf16x8, r);
}
constexpr int SPIN_CAP = 1 << 15;

template <int SET> __device__ __forceinline__ void conv_body(LAS char* pa, LAS char* pb, f32x4 (&acc)[4]) {
    constexpr int T0 = SET ? 6 : 7, T1 = SET ? 5 : 4, T2 = SET ? 2 : 3, T3 = SET ? 1 : 0;
#pragma unroll 1
    for (int r = 0; r < 4; ++r) {
        bf16x8 A[T0 + 1], B[T0 + 1];
#pragma unroll
        for (int a = 0; a <= T0; ++a) { A[a] = *(LAS bf16x8*)(pa + 512 * (7 - a)); B[a] = *(LAS bf16x8*)(pb + 512 * a); }
#pragma unroll
        for (int a = 0; a <= T0; ++a) {
            acc[0] = __builtin_amdgcn_mfma_f32_16x16x32_bf16(A[a], B[T0 - a], acc[0], 0, 0, 0);
            if (a <= T1) acc[1] = __builtin_amdgcn_mfma_f32_16x16x32_bf16(A[a], B[T1 - a], acc[1], 0, 0, 0);
            if (a <= T2) acc[2] = __builtin_amdgcn_mfma_f32_16x16x32_bf16(A[a], B[T2 - a], acc[2], 0, 0, 0);
            if (a <= T3) acc[3] = __builtin_amdgcn_mfma_f32_16x16x32_bf16(A[a], B[T3 - a], acc[3], 0, 0, 0);
        }
        pa -= 64; pb -= 64;
    }
}

__global__ void __launch_bounds__(512, 2)
fused_s4d_mlp(const u16* __restrict__ x, const u16* __restrict__ pLogA, const u16* __restrict__ pAim, const u16* __restrict__ pCr,
              const u16* __restrict__ pCi, const u16* __restrict__ pLogdt, const u16* __restrict__ pDsk, const u16* __restrict__ w1,
              const u16* __restrict__ w2, u16* __restrict__ out, char* ws) {
    extern __shared__ __attribute__((aligned(16))) char smem_raw[];
    LAS char* lds = (LAS char*)smem_raw;
    const u32 lds0 = (u32)(uintptr_t)lds;
    const int tid = threadIdx.x, b = blockIdx.x, lane = tid & 63;
    const int wave = __builtin_amdgcn_readfirstlane(tid >> 6);
    STAMP(0);
    const int xj = b & 7, xi = b >> 3;
    const bool g1on = xi < 28;
    const int rt1 = xj * 2 + (xi >= 14 ? 1 : 0);
    const int ct = xi >= 28 ? xi - 28 : (xi >= 14 ? xi - 14 : xi);
    const int r64 = xj * 4 + (xi & 3), cd = xi >> 2;

    u32 Ev, ldtp, dskp;
    asm volatile("global_load_dword %0, %1, %2 sc1" : "=v"(Ev) : "v"(0u), "s"(ws) : "memory");
    asm volatile("s_load_dword %0, %2, %4\n\ts_load_dword %1, %3, %4"
                 : "=&s"(ldtp), "=&s"(dskp) : "s"(pLogdt), "s"(pDsk), "s"(b * 4) : "memory");
    u32x4 xt, xr, tb0; u32x2 tb1; u32 pa_, pb_, pc_, pd_, warm;
    {
        const u32 o_xt = (u32)(((b >> 1) * 16 + (tid >> 5)) * 1024 + (b & 1) * 512 + (tid & 31) * 16);
        const u32 o_xr = (u32)((b * 8 + wave) * 1024 + lane * 16);
        const u32 o_p = (u32)((b * 256 + (tid & 255)) * 2);
        const u32 o_tb = (u32)((tid & 255) * 32);
        asm volatile("global_load_dwordx4 %0, %1, %2" : "=v"(xt) : "v"(o_xt), "s"(x) : "memory");
        asm volatile("global_load_dwordx4 %0, %1, %2" : "=v"(xr) : "v"(o_xr), "s"(x) : "memory");
        asm volatile("global_load_ushort %0, %1, %2" : "=v"(pa_) : "v"(o_p), "s"(pLogA) : "memory");
        asm volatile("global_load_ushort %0, %1, %2" : "=v"(pb_) : "v"(o_p), "s"(pAim) : "memory");
        asm volatile("global_load_ushort %0, %1, %2" : "=v"(pc_) : "v"(o_p), "s"(pCr) : "memory");
        asm volatile("global_load_ushort %0, %1, %2" : "=v"(pd_) : "v"(o_p), "s"(pCi) : "memory");
        asm volatile("global_load_dwordx4 %0, %1, %2" : "=v"(tb0) : "v"(o_tb), "s"(&g_exptab) : "memory");
        asm volatile("global_load_dwordx2 %0, %1, %2 offset:16" : "=v"(tb1) : "v"(o_tb), "s"(&g_exptab) : "memory");
    }
    const u32 w1v = (u32)((lane >> 4) * 3584 + (((lane & 15) ^ ((lane >> 4) << 2)) * 16));
    const char* w1c = (const char*)w1 + ct * 256;
#pragma unroll
    for (int i = 0; i < 12; ++i) dma16(w1c + (size_t)(wave * 48 + i * 4) * 3584, w1v, lds0 + 65536 + (wave * 12 + i) * 1024);
    {
        const u32 o_w = (u32)(xi * 57344 + (wave * 56 + (lane < 56 ? lane : 55)) * 128);
        asm volatile("global_load_dword %0, %1, %2" : "=v"(warm) : "v"(o_w), "s"(w2) : "memory");
    }
    STAMP(1);
    asm volatile("s_waitcnt vmcnt(20)" : "+v"(xt) :: "memory");
    *(LAS u32x4*)(lds + L_XTILE + (tid >> 5) * 528 + (tid & 31) * 16) = xt;
    asm volatile("s_waitcnt vmcnt(19) lgkmcnt(0)" : "+v"(xr), "+v"(Ev), "+s"(ldtp), "+s"(dskp) :: "memory");
    const u32 tag = (u32)__builtin_amdgcn_readfirstlane((int)Ev) + 1u;
    {
        float v[8] = {bflo(xr.x), bfhi(xr.x), bflo(xr.y), bfhi(xr.y), bflo(xr.z), bfhi(xr.z), bflo(xr.w), bfhi(xr.w)};
        float s = ((v[0] + v[1]) + (v[2] + v[3])) + ((v[4] + v[5]) + (v[6] + v[7]));
        const float mean = wave_sum(s) * (1.f / 512.f);
        float q = 0.f;
#pragma unroll
        for (int k = 0; k < 8; ++k) { float d = v[k] - mean; q = __builtin_fmaf(d, d, q); }
        const float var = wave_sum(q) * (1.f / 512.f);
        const float den = __builtin_amdgcn_sqrtf(rbf(rbf(var) + 1e-5f));
        if (lane == 0) *(LAS u32*)(lds + L_ST + wave * 4) = pkbf(mean, den);
    }
    STAMP(2);
    BAR();
    {
        const int q = lane & 7, p = tid >> 3;
        LAS char* src = lds + L_XTILE + q * 1056 + p * 4;
        u32x4 g0 = {*(LAS u32*)(src), tag, *(LAS u32*)(src + 528), tag};
        u32x4 g1 = {*(LAS u32*)(src + 256), tag, *(LAS u32*)(src + 784), tag};
        const u32 o = WS_XT + (u32)(((b & 1) * 128 + p) * 16384 + ((b >> 1) * 16 + 2 * q) * 8);
        asm volatile("global_store_dwordx4 %0, %1, %2 sc1\n\ts_nop 1" :: "v"(o), "v"(g0), "s"(ws) : "memory");
        asm volatile("global_store_dwordx4 %0, %1, %2 sc1\n\ts_nop 1" :: "v"(o + 64u * 16384u), "v"(g1), "s"(ws) : "memory");
        if (tid < 8) {
            u32x2 g = {*(LAS u32*)(lds + L_ST + tid * 4), tag};
            asm volatile("global_store_dwordx2 %0, %1, %2 sc1" :: "v"(WS_STATS + (u32)(b * 128 + tid * 8)), "v"(g), "s"(ws) : "memory");
        }
    }
    STAMP(3);
    asm volatile("s_waitcnt vmcnt(17)" : "+v"(pa_), "+v"(pb_), "+v"(pc_), "+v"(pd_) :: "memory");
    {
        const int ch = (wave >> 1) & 1;
        const float dt = rbf(expf(ch ? bfhi(ldtp) : bflo(ldtp)));
        const float zr = -rbf(expf(bflo(pa_))) * dt, zi = bflo(pb_) * dt;
        LAS char* tp = lds + L_TAB + ch * TCH + ((wave & 1) * 64 + lane) * 4;
        if (wave < 4) {
            tp += 21 * TROW;
            const cplx w = cexp_m(zr, zi, 8.f), w2c = cmul(w, w);
            cplx e0 = {1.f, 0.f}, e1 = w;
#pragma unroll
            for (int j = 0; j < 32; j += 2) {
                *(LAS u32*)(tp + j * TROW) = pkh(e0.r, e0.i);
                *(LAS u32*)(tp + (j + 1) * TROW) = pkh(e1.r, e1.i);
                if (j < 30) { e0 = cmul(e0, w2c); e1 = cmul(e1, w2c); }
            }
        } else {
            const float cr = bflo(pc_), ci = bflo(pd_);
            const u32 eb = (__float_as_uint(wave_max(fmaxf(__builtin_fabsf(cr), __builtin_fabsf(ci)))) >> 23) & 0xffu;
            const float sc = __uint_as_float((254u - eb) << 23);
            if (lane == 0) *(LAS float*)(lds + L_SC + (wave & 3) * 4) = 2.f * dt * __uint_as_float(eb << 23);
            const cplx w1c_ = cexp_m(zr, zi, 1.f), wq = cexp_m(zr, zi, 256.f);
            cplx v[8]; v[0] = {cr * sc, ci * sc};
#pragma unroll
            for (int k = 1; k < 8; ++k) v[k] = cmul(v[k - 1], w1c_);
#pragma unroll
            for (int k = 0; k < 8; ++k) *(LAS u32*)(tp + k * TROW) = pkh(v[k].r, -v[k].i);
            cplx a0 = cmul(v[0], wq), a2 = cmul(v[2], wq), a4 = cmul(v[4], wq), a6 = cmul(v[6], wq);
            *(LAS u32*)(tp + 8 * TROW) = pkh(a0.r, -a0.i); *(LAS u32*)(tp + 9 * TROW) = pkh(a2.r, -a2.i);
            *(LAS u32*)(tp + 10 * TROW) = pkh(a4.r, -a4.i); *(LAS u32*)(tp + 11 * TROW) = pkh(a6.r, -a6.i);
            a0 = cmul(a0, wq); a4 = cmul(a4, wq);
            *(LAS u32*)(tp + 12 * TROW) = pkh(a0.r, -a0.i); *(LAS u32*)(tp + 13 * TROW) = pkh(a4.r, -a4.i);
            a0 = cmul(a0, wq); a4 = cmul(a4, wq);
            *(LAS u32*)(tp + 14 * TROW) = pkh(a0.r, -a0.i); *(LAS u32*)(tp + 15 * TROW) = pkh(a4.r, -a4.i);
#pragma unroll
            for (int k = 16; k < 21; ++k) { a0 = cmul(a0, wq); *(LAS u32*)(tp + k * TROW) = pkh(a0.r, -a0.i); }
        }
    }
    STAMP(4);
    BAR();
    {
        const int ch = wave >> 2, rtile = (wave >> 1) & 1, ctile = wave & 1, fr = lane & 15, g = lane >> 4;
        const int arow = min(16 * rtile + fr, 20);
        LAS char* pa = lds + L_TAB + ch * TCH + arow * TROW + g * 16;
        LAS char* pb = lds + L_TAB + ch * TCH + (21 + 16 * ctile + fr) * TROW + g * 16;
        f32x4 d0 = {0, 0, 0, 0}, d1 = {0, 0, 0, 0};
#pragma unroll
        for (int ks = 0; ks < 4; ++ks) {
            d0 = __builtin_amdgcn_mfma_f32_16x16x32_f16(*(LAS f16x8*)(pa + ks * 64), *(LAS f16x8*)(pb + ks * 64), d0, 0, 0, 0);
            d1 = __builtin_amdgcn_mfma_f32_16x16x32_f16(*(LAS f16x8*)(pa + 256 + ks * 64), *(LAS f16x8*)(pb + 256 + ks * 64), d1, 0, 0, 0);
        }
        const f32x2 sc = *(LAS f32x2*)(lds + L_SC + ch * 8);
        LAS char* tp = lds + L_T + ch * 3072 + (16 * rtile + 4 * g) * 144 + (16 * ctile + fr) * 4;
#pragma unroll
        for (int i = 0; i < 4; ++i)
            if (16 * rtile + 4 * g + i <= 20) *(LAS float*)(tp + i * 144) = d0[i] * sc.x + d1[i] * sc.y;
        if (tid == 0 || tid == 256) *(LAS float*)(lds + L_T + ch * 3072 + 128) = 0.f;
    }
    BAR();
    STAMP(5);
    {
        asm volatile("s_waitcnt vmcnt(0)" : "+v"(tb0), "+v"(tb1), "+v"(warm) :: "memory");
        const int ch = tid >> 8, mu = tid & 255;
        LAS char* tp = lds + L_T + ch * 3072;
        const u32 o[11] = {tb0.x & 0xffffu, tb0.x >> 16, tb0.y & 0xffffu, tb0.y >> 16, tb0.z & 0xffffu, tb0.z >> 16,
                           tb0.w & 0xffffu, tb0.w >> 16, tb1.x & 0xffffu, tb1.x >> 16, tb1.y & 0xffffu};
        float V[11];
#pragma unroll
        for (int k = 0; k < 11; ++k) V[k] = *(LAS float*)(tp + o[k]);
        u32 P[11];
#pragma unroll
        for (int k = 1; k < 11; ++k) P[k] = pkbf(V[k], V[k - 1]);
        LAS char* kp = lds + L_KC + ch * 16640 + (256 - mu) * 16;
#pragma unroll
        for (int a = 0; a < 4; ++a) { u32x4 c = {P[7 + a], P[5 + a], P[3 + a], P[1 + a]}; *(LAS u32x4*)(kp + a * 4160) = c; }
        const u32x4 z = {0, 0, 0, 0};
        if (tid < 24) *(LAS u32x4*)(lds + L_KC + (tid / 3) * 4160 + (257 + tid % 3) * 16) = z;
        if (tid >= 64 && tid < 192) { const int k = tid - 64; *(LAS u32x4*)(lds + L_UC + (k >> 5) * 4640 + (k & 31) * 16) = z; }
        if (tid >= 192 && tid < 194) { const u32x2 z2 = {0, 0}; *(LAS u32x2*)(lds + L_UC + (tid - 192) * 9280 + 512) = z2; }
    }
    STAMP(6);
    float xs[2][4], ud[2][4];
    {
        u32x4 a0, a1, s0, s1;
        const u32 ox = WS_XT + (u32)(b * 16384 + wave * 2048 + lane * 16);
        const u32 os = WS_STATS + (u32)((wave * 32 + (lane >> 2)) * 128 + (lane & 3) * 16);
        for (int spin = 0; spin < SPIN_CAP; ++spin) {
            asm volatile("global_load_dwordx4 %0, %4, %6 sc1\n\tglobal_load_dwordx4 %1, %4, %6 offset:1024 sc1\n\t"
                         "global_load_dwordx4 %2, %5, %6 sc1\n\tglobal_load_dwordx4 %3, %5, %6 offset:2048 sc1\n\ts_waitcnt vmcnt(0)"
                         : "=&v"(a0), "=&v"(a1), "=&v"(s0), "=&v"(s1) : "v"(ox), "v"(os), "s"(ws) : "memory");
            const bool ok = a0.y == tag && a0.w == tag && a1.y == tag && a1.w == tag && s0.y == tag && s0.w == tag && s1.y == tag && s1.w == tag;
            if (__all(ok)) break;
            __builtin_amdgcn_s_sleep(8);
        }
        STAMP(7);
        const u32 xv[4] = {a0.x, a0.z, a1.x, a1.z}, sv[4] = {s0.x, s0.z, s1.x, s1.z};
        const float D0 = bflo(dskp), D1 = bfhi(dskp);
        float u0[4], u1[4];
#pragma unroll
        for (int k = 0; k < 4; ++k) {
            const float m = bflo(sv[k]), ri = __builtin_amdgcn_rcpf(bfhi(sv[k]));
            xs[0][k] = bflo(xv[k]); xs[1][k] = bfhi(xv[k]);
            const u32 dm = pkbf(xs[0][k] - m, xs[1][k] - m);
            u0[k] = bflo(dm) * ri; u1[k] = bfhi(dm) * ri;
        }
        LAS char* up = lds + L_UC + (wave * 256 + 2 * lane) * 2;
#pragma unroll
        for (int gq = 0; gq < 2; ++gq) {
            const u32 c0 = pkbf(u0[2 * gq], u0[2 * gq + 1]), c1 = pkbf(u1[2 * gq], u1[2 * gq + 1]);
            *(LAS u32*)(up + gq * 256 + 520) = c0;  *(LAS u32*)(up + gq * 256 + 4640 + 512) = c0;
            *(LAS u32*)(up + gq * 256 + 9280 + 520) = c1;  *(LAS u32*)(up + gq * 256 + 9280 + 4640 + 512) = c1;
            ud[0][2 * gq] = bflo(c0) * D0; ud[0][2 * gq + 1] = bfhi(c0) * D0;
            ud[1][2 * gq] = bflo(c1) * D1; ud[1][2 * gq + 1] = bfhi(c1) * D1;
        }
    }
    BAR();
    STAMP(8);
    {
        const int ch = wave >> 2, rh = (wave >> 1) & 1, set = wave & 1, fr = lane & 15, g = lane >> 4;
        LAS char* pa = lds + L_KC + ch * 16640 + (fr & 3) * 4160 + 2 * (2048 - 8 * (fr >> 2) + 8 * g) - 3584 - rh * 256;
        LAS char* pb = lds + L_UC + ch * 9280 + (fr & 1) * 4640 + 2 * (256 + 32 * (fr >> 1) + 8 * g) - rh * 256;
        f32x4 acc[4] = {};
        if (set) conv_body<1>(pa, pb, acc); else conv_body<0>(pa, pb, acc);
        STAMP(9);
        BAR();
        const int blk = fr >> 1;
        LAS char* yp = lds + L_Y + (rh * 2 + ch) * 8192 + blk * 128 + (((2 * g + (fr & 1)) ^ (2 * (blk & 3))) * 16);
        *(LAS f32x4*)(yp + (set ? 6 : 7) * 1024) = acc[0];
        *(LAS f32x4*)(yp + (set ? 5 : 4) * 1024) = acc[1];
        *(LAS f32x4*)(yp + (set ? 2 : 3) * 1024) = acc[2];
        *(LAS f32x4*)(yp + (set ? 1 : 0) * 1024) = acc[3];
    }
#pragma unroll
    for (int i = 0; i < 4; ++i) dma16(w1c + (size_t)(384 + wave * 16 + i * 4) * 3584, w1v, lds0 + 32768 + (wave * 4 + i) * 1024);
    BAR();
    {
        const int blk = lane >> 4;
        LAS char* yp = lds + L_Y + wave * 1024 + blk * 128 + ((((lane >> 1) & 7) ^ (2 * blk)) * 16) + (lane & 1) * 8;
#pragma unroll
        for (int gq = 0; gq < 2; ++gq)
#pragma unroll
            for (int c = 0; c < 2; ++c) {
                const f32x2 p = *(LAS f32x2*)(yp + gq * 512 + c * 8192), q = *(LAS f32x2*)(yp + gq * 512 + c * 8192 + 16384);
                const u32 yb = pkbf(p.x + q.x, p.y + q.y);
                const u32 ab = pkbf(bflo(yb) + rbf(ud[c][2 * gq]), bfhi(yb) + rbf(ud[c][2 * gq + 1]));
                const u32 hb = pkbf(xs[c][2 * gq] + bflo(ab), xs[c][2 * gq + 1] + bfhi(ab));
                const u32 o = WS_H1 + (u32)(((wave * 2 + gq) * 512 + b * 2 + c) * 256 + lane * 4);
                asm volatile("global_store_dword %0, %1, %2 sc1" :: "v"(o), "v"(hb), "s"(ws) : "memory");
            }
    }
    STAMP(10);
    VMW(0);
    BAR();
    if (tid == 0) asm volatile("global_store_dword %0, %1, %2 sc1" :: "v"(WS_FLAG1 + (u32)b * 128u), "v"(tag), "s"(ws) : "memory");
    STAMP(11);
    const int fm = wave & 3, tn = wave >> 2;
    const int tq = (lane & 15) >> 2, tp_ = lane & 3, tG = (lane >> 4) & 1, th = lane >> 5;
    bf16x8 WA[32];
    f32x16 cs = {};
    {
        const u32 lo = (u32)((8 * th + tq) * 256 + (((4 * fm + 2 * tG + (tp_ >> 1)) ^ (tq << 2)) * 16) + 8 * (tp_ & 1));
#pragma unroll
        for (int i = 0; i < 32; ++i) {
            const int ks = (i & ~3) | ((i + fm) & 3);
            const u32 a = lo + (u32)ks * 4096u + (i < 24 ? 65536u : (u32)-65536);
            WA[i] = trfrag(lds + a, lds + a + 1024);
        }
        const u32x4 one4 = {0x3f803f80u, 0x3f803f80u, 0x3f803f80u, 0x3f803f80u};
        const bf16x8 ones = __builtin_bit_cast(bf16x8, one4);
        if (tn == 0) {
#pragma unroll
            for (int i = 0; i < 16; ++i) cs = __builtin_amdgcn_mfma_f32_32x32x16_bf16(WA[i], ones, cs, 0, 0, 0);
        } else {
#pragma unroll
            for (int i = 16; i < 32; ++i) cs = __builtin_amdgcn_mfma_f32_32x32x16_bf16(WA[i], ones, cs, 0, 0, 0);
        }
    }
    STAMP(12);
    if (wave == 0) {
        const u32 of = WS_FLAG1 + (u32)lane * 128u;
        for (int spin = 0; spin < SPIN_CAP; ++spin) {
            u32 f0, f1, f2, f3;
            asm volatile("global_load_dword %0, %4, %6 sc1\n\tglobal_load_dword %1, %5, %6 sc1\n\t"
                         "global_load_dword %2, %4, %7 sc1\n\tglobal_load_dword %3, %5, %7 sc1\n\ts_waitcnt vmcnt(0)"
                         : "=&v"(f0), "=&v"(f1), "=&v"(f2), "=&v"(f3) : "v"(of), "v"(of + 8192u), "s"(ws), "s"(ws + 16384) : "memory");
            if (__all(f0 == tag && f1 == tag && f2 == tag && f3 == tag)) break;
            __builtin_amdgcn_s_sleep(4);
        }
        if (b == 0 && lane == 0) asm volatile("global_store_dword %0, %1, %2 sc1" :: "v"(0u), "v"(tag), "s"(ws) : "memory");
        VMW(0);
    }
    BAR();
    STAMP(13);
    if ((lane & 31) == 0) {
#pragma unroll
        for (int k = 0; k < 4; ++k) {
            const f32x4 c4 = {cs[4 * k], cs[4 * k + 1], cs[4 * k + 2], cs[4 * k + 3]};
            *(LAS f32x4*)(lds + L_SCR + 4096 + tn * 512 + (fm * 32 + 8 * k + 4 * th) * 4) = c4;
        }
    }
    const int kq = wave >> 1, tt = wave & 1;
    u32 res[8];
    const u32 w2v = (u32)((lane >> 3) * 1024 + (((lane & 7) ^ (((lane >> 4) & 1) << 2)) * 16));
    const char* w2c = (const char*)w2 + cd * 128 + (size_t)(kq * 448 + tt * 8) * 1024;
    {
        f32x16 acc0 = {}, acc1 = {};
        float sm0 = 0.f, sq0 = 0.f, sm1 = 0.f, sq1 = 0.f;
        const char* hsrc = ws + WS_H1 + (size_t)rt1 * 131072 + wave * 2048;
        const u32 hv_ = (u32)((lane >> 4) * 256 + (((lane & 15) ^ ((lane >> 4) << 2)) * 16));
        const u32 lb = (u32)((8 * th + tq) * 256 + (((8 * tn + 2 * tG + (tp_ >> 1)) ^ (tq << 2)) * 16) + 8 * (tp_ & 1));
        const bf16x2 one2 = __builtin_bit_cast(bf16x2, 0x3f803f80u);
        if (g1on) {
#pragma unroll
            for (int c = 0; c < 4; ++c) { dma16_dev(hsrc + c * 16384, hv_, lds0 + c * 16384 + wave * 2048); dma16_dev(hsrc + c * 16384 + 1024, hv_, lds0 + c * 16384 + wave * 2048 + 1024); }
        }
#pragma unroll
        for (int c = 0; c < 8; ++c) {
            if (c <= 4) VMW(6); else if (c == 5) VMW(4 + 11); else if (c == 6) VMW(2 + 11); else VMW(11);
            BAR();
            if (c == 0) { STAMP(14); }
            if (c < 4) {
                if (g1on) { dma16_dev(hsrc + (c + 4) * 16384, hv_, lds0 + (c + 4) * 16384 + wave * 2048); dma16_dev(hsrc + (c + 4) * 16384 + 1024, hv_, lds0 + (c + 4) * 16384 + wave * 2048 + 1024); }
            }
            if (c == 4) {
                const u32 orr = WS_H1 + (u32)(((r64 >> 1) * 512 + cd * 64 + kq * 16 + 4 * th) * 256 + ((r64 & 1) * 64 + tt * 32 + (lane & 31)) * 2);
#pragma unroll
                for (int k = 0; k < 8; ++k)
                    asm volatile("global_load_ushort %0, %1, %2 offset:%3 sc1" : "=v"(res[k]) : "v"(orr), "s"(ws), "i"((k & 3) * 256 + (k >> 2) * 2048) : "memory");
#pragma unroll
                for (int s = 0; s < 3; ++s) dma16(w2c + s * 16384, w2v, lds0 + 131072 + s * 8192 + wave * 1024);
            }
            if (g1on) {
#pragma unroll
                for (int r = 0; r < 4; ++r) {
                    const int i = 4 * c + r;
                    const u32 a = lb + (u32)(4 * c + ((r + fm) & 3)) * 4096u;
                    const bf16x8 B0 = trfrag(lds + a, lds + a + 1024), B1 = trfrag(lds + (a ^ 64u), lds + (a ^ 64u) + 1024);
                    acc0 = __builtin_amdgcn_mfma_f32_32x32x16_bf16(WA[i], B0, acc0, 0, 0, 0);
                    acc1 = __builtin_amdgcn_mfma_f32_32x32x16_bf16(WA[i], B1, acc1, 0, 0, 0);
                    if (r == 0) {
                        const u32x4 q0 = __builtin_bit_cast(u32x4, B0), q1 = __builtin_bit_cast(u32x4, B1);
#pragma unroll
                        for (int k = 0; k < 4; ++k) {
                            const u32 d0 = q0[k], d1 = q1[k];
                            const bf16x2 e0 = __builtin_bit_cast(bf16x2, d0), e1 = __builtin_bit_cast(bf16x2, d1);
                            sm0 = __builtin_amdgcn_fdot2_f32_bf16(e0, one2, sm0, false); sq0 = __builtin_amdgcn_fdot2_f32_bf16(e0, e0, sq0, false);
                            sm1 = __builtin_amdgcn_fdot2_f32_bf16(e1, one2, sm1, false); sq1 = __builtin_amdgcn_fdot2_f32_bf16(e1, e1, sq1, false);
                        }
                    }
                }
            }
        }
        STAMP(15);
        {
            auto x0 = __builtin_amdgcn_permlane32_swap(__float_as_uint(sm0), __float_as_uint(sm1), false, false);
            auto x1 = __builtin_amdgcn_permlane32_swap(__float_as_uint(sq0), __float_as_uint(sq1), false, false);
            const f32x2 pr = {__uint_as_float(x0[0]) + __uint_as_float(x0[1]), __uint_as_float(x1[0]) + __uint_as_float(x1[1])};
            *(LAS f32x2*)(lds + L_SCR + (tn * 4 + fm) * 512 + lane * 8) = pr;
        }
        BAR();
        if (g1on) {
            float mu_[2], ri_[2];
#pragma unroll
            for (int u = 0; u < 2; ++u) {
                LAS char* sp = lds + L_SCR + tn * 2048 + (u * 32 + (lane & 31)) * 8;
                const f32x2 p0 = *(LAS f32x2*)(sp), p1 = *(LAS f32x2*)(sp + 512), p2 = *(LAS f32x2*)(sp + 1024), p3 = *(LAS f32x2*)(sp + 1536);
                const float mean = ((p0.x + p1.x) + (p2.x + p3.x)) * (1.f / 512.f);
                const float var = fmaxf(((p0.y + p1.y) + (p2.y + p3.y)) * (1.f / 512.f) - mean * mean, 0.f);
                mu_[u] = rbf(mean);
                ri_[u] = __builtin_amdgcn_rcpf(rbf(__builtin_amdgcn_sqrtf(rbf(rbf(var) + 1e-5f))));
            }
            float cf[16];
#pragma unroll
            for (int k = 0; k < 4; ++k) {
                LAS char* cp = lds + L_SCR + 4096 + (fm * 32 + 8 * k + 4 * th) * 4;
                const f32x4 c0 = *(LAS f32x4*)(cp), c1 = *(LAS f32x4*)(cp + 512);
#pragma unroll
                for (int e = 0; e < 4; ++e) cf[4 * k + e] = c0[e] + c1[e];
            }
#pragma unroll
            for (int u = 0; u < 2; ++u)
#pragma unroll
                for (int s = 0; s < 2; ++s) {
                    u32x4 hq;
#pragma unroll
                    for (int k = 0; k < 4; ++k) {
                        const int r = 8 * s + 2 * k;
                        const float a0 = (u ? acc1[r] : acc0[r]), a1 = (u ? acc1[r + 1] : acc0[r + 1]);
                        hq[k] = pkbf(fmaxf((a0 - mu_[u] * cf[r]) * ri_[u], 0.f), fmaxf((a1 - mu_[u] * cf[r + 1]) * ri_[u], 0.f));
                    }
                    const int ks = ct * 8 + fm * 2 + s, q = ks / 28;
                    const char* dst = ws + WS_HH + (size_t)((((rt1 * 2 + tn) * 4 + q) * 2 + u) * 28 + (ks - q * 28)) * 1024;
                    asm volatile("global_store_dwordx4 %0, %1, %2 sc1\n\ts_nop 1" :: "v"((u32)lane * 16u), "v"(hq), "s"(dst) : "memory");
                }
        }
    }
#pragma unroll
    for (int s = 3; s < 19; ++s) dma16(w2c + s * 16384, w2v, lds0 + (s - 3) * 8192 + wave * 1024);
    VMW(16);
    BAR();
    if (g1on && tid == 0) asm volatile("global_store_dword %0, %1, %2 sc1" :: "v"(WS_FLAG2 + (u32)(rt1 * 14 + ct) * 128u), "v"(tag), "s"(ws) : "memory");
    STAMP(16);
    const u32 la = (u32)(kq * 2048 + (4 * th + tq) * 128 + (((2 * tG + (tp_ >> 1)) ^ ((tq >> 1) << 2)) * 16) + 8 * (tp_ & 1));
    bf16x8 WB[9][2];
    VMW(10);
    BAR();
#pragma unroll
    for (int i = 0; i < 9; ++i) {
        const u32 a = la + (i < 3 ? 131072u + i * 8192u : (i - 3) * 8192u);
        WB[i][0] = trfrag(lds + a, lds + a + 1024); WB[i][1] = trfrag(lds + (a ^ 64u), lds + (a ^ 64u) + 1024);
    }
    BAR();
#pragma unroll
    for (int s = 19; s < 28; ++s) dma16(w2c + s * 16384, w2v, lds0 + (s - 19 < 3 ? 131072 + (s - 19) * 8192 : (s - 22) * 8192) + wave * 1024);
    if (wave == 0) {
        const u32 of = WS_FLAG2 + (u32)((r64 >> 1) * 14 + (lane < 14 ? lane : 0)) * 128u;
        for (int spin = 0; spin < SPIN_CAP; ++spin) {
            u32 f0;
            asm volatile("global_load_dword %0, %1, %2 sc1\n\ts_waitcnt vmcnt(0)" : "=&v"(f0) : "v"(of), "s"(ws) : "memory");
            if (__all(f0 == tag)) break;
            __builtin_amdgcn_s_sleep(4);
        }
    }
    asm volatile("s_waitcnt vmcnt(0)" : "+v"(res[0]), "+v"(res[1]), "+v"(res[2]), "+v"(res[3]), "+v"(res[4]), "+v"(res[5]), "+v"(res[6]), "+v"(res[7]) :: "memory");
    BAR();
    STAMP(17);
    f32x16 o0 = {}, o1 = {};
    {
        constexpr int PF = 12;
        const char* hsrc = ws + WS_HH + (size_t)((r64 * 4 + kq) * 2 + tt) * 28672;
        const u32 vo = (u32)lane * 16u;
        u32x4 hv[28];
#pragma unroll
        for (int i = 0; i < PF; ++i) asm volatile("global_load_dwordx4 %0, %1, %2 offset:%3 sc1" : "=v"(hv[i]) : "v"(vo), "s"(hsrc + (i >> 2) * 4096), "i"((i & 3) * 1024) : "memory");
#pragma unroll
        for (int i = 0; i < 28; ++i) {
            asm volatile("s_waitcnt vmcnt(%1)" : "+v"(hv[i]) : "i"(i + PF <= 28 ? PF - 1 : 27 - i) : "memory");
            if (i + PF < 28) asm volatile("global_load_dwordx4 %0, %1, %2 offset:%3 sc1" : "=v"(hv[i + PF]) : "v"(vo), "s"(hsrc + ((i + PF) >> 2) * 4096), "i"(((i + PF) & 3) * 1024) : "memory");
            bf16x8 A0, A1;
            if (i < 9) { A0 = WB[i][0]; A1 = WB[i][1]; }
            else {
                const u32 a = la + (i < 19 ? (i - 3) * 8192u : (i - 19 < 3 ? 131072u + (i - 19) * 8192u : (i - 22) * 8192u));
                A0 = trfrag(lds + a, lds + a + 1024); A1 = trfrag(lds + (a ^ 64u), lds + (a ^ 64u) + 1024);
            }
            const bf16x8 Bf = __builtin_bit_cast(bf16x8, hv[i]);
            o0 = __builtin_amdgcn_mfma_f32_32x32x16_bf16(A0, Bf, o0, 0, 0, 0);
            o1 = __builtin_amdgcn_mfma_f32_32x32x16_bf16(A1, Bf, o1, 0, 0, 0);
        }
    }
    STAMP(18);
    BAR();
    {
        LAS char* xp = lds + tt * 32768 + kq * 2048 + lane * 16;
#pragma unroll
        for (int dq = 0; dq < 4; ++dq) {
            const f32x16& o = (dq >> 1) ? o1 : o0;
            const int r = 8 * (dq & 1);
            const f32x4 v0 = {o[r], o[r + 1], o[r + 2], o[r + 3]}, v1 = {o[r + 4], o[r + 5], o[r + 6], o[r + 7]};
            *(LAS f32x4*)(xp + dq * 8192) = v0; *(LAS f32x4*)(xp + dq * 8192 + 1024) = v1;
        }
        BAR();
        LAS char* rp = lds + tt * 32768 + kq * 8192 + lane * 16;
        f32x4 s0 = *(LAS f32x4*)(rp), s1 = *(LAS f32x4*)(rp + 1024);
#pragma unroll
        for (int s = 1; s < 4; ++s) { s0 += *(LAS f32x4*)(rp + s * 2048); s1 += *(LAS f32x4*)(rp + s * 2048 + 1024); }
        u32 p0 = pkbf(s0[0], s0[1]), p1 = pkbf(s0[2], s0[3]), p2 = pkbf(s1[0], s1[1]), p3 = pkbf(s1[2], s1[3]);
        p0 = pkbf(bflo(p0) + bflo(res[0]), bfhi(p0) + bflo(res[1])); p1 = pkbf(bflo(p1) + bflo(res[2]), bfhi(p1) + bflo(res[3]));
        p2 = pkbf(bflo(p2) + bflo(res[4]), bfhi(p2) + bflo(res[5])); p3 = pkbf(bflo(p3) + bflo(res[6]), bfhi(p3) + bflo(res[7]));
        auto y0 = __builtin_amdgcn_permlane32_swap(p0, p2, false, false);
        auto y1 = __builtin_amdgcn_permlane32_swap(p1, p3, false, false);
        const u32x4 ov = {y0[0], y1[0], y0[1], y1[1]};
        *(u32x4*)(out + (size_t)(r64 * 64 + tt * 32 + (lane & 31)) * 512 + cd * 64 + kq * 16 + th * 8) = ov;
    }
    STAMP(19);
}

extern "C" void kernel_launch(void* const* d_in, const int* in_sizes, int n_in, void* d_out, int out_size, void* d_ws, size_t ws_size,
                              hipStream_t stream) {
    (void)hipFuncSetAttribute((const void*)fused_s4d_mlp, hipFuncAttributeMaxDynamicSharedMemorySize, LDS_BYTES);
    fused_s4d_mlp<<<256, 512, LDS_BYTES, stream>>>((const u16*)d_in[0], (const u16*)d_in[1], (const u16*)d_in[2], (const u16*)d_in[3],
                                                    (const u16*)d_in[4], (const u16*)d_in[5], (const u16*)d_in[6], (const u16*)d_in[8],
                                                    (const u16*)d_in[9], (u16*)d_out, (char*)d_ws);
}
```

```cpp
#include <hip/hip_runtime.h>
#include <stdint.h>

typedef unsigned short u16;
typedef unsigned u32;
typedef u32 u32x4 __attribute__((ext_vector_type(4)));
typedef u32 u32x2 __attribute__((ext_vector_type(2)));
typedef float f32x2 __attribute__((ext_vector_type(2)));
typedef float f32x4 __attribute__((ext_vector_type(4)));
typedef float f32x16 __attribute__((ext_vector_type(16)));
typedef __bf16 bf16x2 __attribute__((ext_vector_type(2)));
typedef __bf16 bf16x8 __attribute__((ext_vector_type(8)));
typedef _Float16 f16x2 __attribute__((ext_vector_type(2)));
typedef _Float16 f16x8 __attribute__((ext_vector_type(8)));
typedef short s16x4 __attribute__((ext_vector_type(4)));
#define LAS __attribute__((address_space(3)))

constexpr u32 WS_EPOCH = 0;
constexpr u32 WS_STATS = 4096;
constexpr u32 WS_FLAG1 = 40960;
constexpr u32 WS_FLAG2 = 77824;
constexpr u32 WS_H1    = 131072;
constexpr u32 WS_XT    = WS_H1 + (2u << 20);
constexpr u32 WS_HH    = WS_XT + (4u << 20);
constexpr u32 WS_STAMP = 200u << 20;

constexpr u32 L_TAB   = 0;
constexpr u32 TROW = 528, TCH = 53 * 528;
constexpr u32 L_KC    = 0;
constexpr u32 L_UC    = 33280;
constexpr u32 L_XTILE = 55968;
constexpr u32 L_T     = 56064;
constexpr u32 L_SC    = 64448;
constexpr u32 L_ST    = 64480;
constexpr u32 L_Y     = 0;
constexpr u32 L_SCR   = 155648;
constexpr u32 LDS_BYTES = 163840;

struct ExpTab { u16 v[256 * 16]; };
constexpr ExpTab make_exptab() {
    ExpTab t{};
    for (int mu = 0; mu < 256; ++mu)
        for (int k = 0; k < 16; ++k) {
            int l = 8 * mu + k - 3, off = 128;
            if (k < 11 && l >= 0) {
                int s = (l >= 1024) + (l >= 512) + (l >= 256);
                int pos = s ? (((l + (1 << (s - 1)) - 1 + ((l >> s) & 1)) >> s) << s) : l;
                int i = pos >> 8, col = (pos >> 3) & 31, bb = pos & 7;
                int row = i == 0 ? bb : i == 1 ? 8 + bb / 2 : i < 4 ? 12 + 2 * (i - 2) + bb / 4 : 12 + i;
                off = row * 144 + col * 4;
            }
            t.v[mu * 16 + k] = (u16)off;
        }
    return t;
}
static __device__ const ExpTab g_exptab = make_exptab();

#define BAR() asm volatile("s_waitcnt lgkmcnt(0)\n\ts_barrier" ::: "memory")
#define VMW(n) asm volatile("s_waitcnt vmcnt(%0)" :: "i"(n) : "memory")
#define STAMPS 1
#define STAMP(k) st[k] = __builtin_amdgcn_s_memrealtime()

__device__ __forceinline__ float bflo(u32 v) { return __uint_as_float(v << 16); }
__device__ __forceinline__ float bfhi(u32 v) { return __uint_as_float(v & 0xffff0000u); }
__device__ __forceinline__ u32 pkbf(float a, float b) { f32x2 x = {a, b}; bf16x2 r = __builtin_convertvector(x, bf16x2); return __builtin_bit_cast(u32, r); }
__device__ __forceinline__ u32 pkh(float a, float b) { f32x2 x = {a, b}; f16x2 r = __builtin_convertvector(x, f16x2); return __builtin_bit_cast(u32, r); }
__device__ __forceinline__ float rbf(float a) { return bflo(pkbf(a, a)); }
template <int CTRL> __device__ __forceinline__ float dpp_add(float v) {
    return v + __int_as_float(__builtin_amdgcn_update_dpp(0, __float_as_int(v), CTRL, 0xf, 0xf, true));
}
template <int CTRL> __device__ __forceinline__ float dpp_max(float v) {
    return fmaxf(v, __int_as_float(__builtin_amdgcn_update_dpp(0, __float_as_int(v), CTRL, 0xf, 0xf, true)));
}
__device__ __forceinline__ float wave_sum(float v) {
    v = dpp_add<0xb1>(v); v = dpp_add<0x4e>(v); v = dpp_add<0x141>(v); v = dpp_add<0x140>(v);
    int i = __float_as_int(v);
    return (__int_as_float(__builtin_amdgcn_readlane(i, 0)) + __int_as_float(__builtin_amdgcn_readlane(i, 16))) +
           (__int_as_float(__builtin_amdgcn_readlane(i, 32)) + __int_as_float(__builtin_amdgcn_readlane(i, 48)));
}
__device__ __forceinline__ float wave_max(float v) {
    v = dpp_max<0xb1>(v); v = dpp_max<0x4e>(v); v = dpp_max<0x141>(v); v = dpp_max<0x140>(v);
    int i = __float_as_int(v);
    return fmaxf(fmaxf(__int_as_float(__builtin_amdgcn_readlane(i, 0)), __int_as_float(__builtin_amdgcn_readlane(i, 16))),
                 fmaxf(__int_as_float(__builtin_amdgcn_readlane(i, 32)), __int_as_float(__builtin_amdgcn_readlane(i, 48))));
}
__device__ __forceinline__ void dma16(const void* sbase, u32 voff, u32 lds) {
    asm volatile("s_mov_b32 m0, %2\n\ts_nop 0\n\tglobal_load_lds_dwordx4 %0, %1" :: "v"(voff), "s"(sbase), "s"(lds) : "memory");
}
__device__ __forceinline__ void dma16_dev(const void* sbase, u32 voff, u32 lds) {
    asm volatile("s_mov_b32 m0, %2\n\ts_nop 0\n\tglobal_load_lds_dwordx4 %0, %1 sc1" :: "v"(voff), "s"(sbase), "s"(lds) : "memory");
}
struct cplx { float r, i; };
__device__ __forceinline__ cplx cmul(cplx a, cplx b) { return {a.r * b.r - a.i * b.i, a.r * b.i + a.i * b.r}; }
__device__ __forceinline__ cplx cexp_m(float zr, float zi, float m) {
    float mag = __builtin_amdgcn_exp2f(zr * m * 1.44269504088896341f);
    float p = zi * m;
    float k = __builtin_rintf(p * 0.15915494309189535f);
    float f = __builtin_fmaf(p, 0.15915494309189535f, -k) + p * 6.4206383e-9f;
    return {mag * __builtin_amdgcn_cosf(f), mag * __builtin_amdgcn_sinf(f)};
}
__device__ __forceinline__ bf16x8 trfrag(LAS char* p0, LAS char* p1) {
    s16x4 a = __builtin_amdgcn_ds_read_tr16_b64_v4i16((LAS s16x4*)p0);
    s16x4 b = __builtin_amdgcn_ds_read_tr16_b64_v4i16((LAS s16x4*)p1);
    u32x2 ua = __builtin_bit_cast(u32x2, a), ub = __builtin_bit_cast(u32x2, b);
    u32x4 r = {ua.x, ua.y, ub.x, ub.y};
    return __builtin_bit_cast(bf16x8, r);
}
constexpr int SPIN_CAP = 1 << 15;

template <int SET> __device__ __forceinline__ void conv_body(LAS char* pa, LAS char* pb, f32x4 (&acc)[4]) {
    constexpr int T0 = SET ? 6 : 7, T1 = SET ? 5 : 4, T2 = SET ? 2 : 3, T3 = SET ? 1 : 0;
#pragma unroll 1
    for (int r = 0; r < 4; ++r) {
        bf16x8 A[T0 + 1], B[T0 + 1];
#pragma unroll
        for (int a = 0; a <= T0; ++a) { A[a] = *(LAS bf16x8*)(pa + 512 * (7 - a)); B[a] = *(LAS bf16x8*)(pb + 512 * a); }
#pragma unroll
        for (int a = 0; a <= T0; ++a) {
            acc[0] = __builtin_amdgcn_mfma_f32_16x16x32_bf16(A[a], B[T0 - a], acc[0], 0, 0, 0);
            if (a <= T1) acc[1] = __builtin_amdgcn_mfma_f32_16x16x32_bf16(A[a], B[T1 - a], acc[1], 0, 0, 0);
            if (a <= T2) acc[2] = __builtin_amdgcn_mfma_f32_16x16x32_bf16(A[a], B[T2 - a], acc[2], 0, 0, 0);
            if (a <= T3) acc[3] = __builtin_amdgcn_mfma_f32_16x16x32_bf16(A[a], B[T3 - a], acc[3], 0, 0, 0);
        }
        pa -= 64; pb -= 64;
    }
}

__global__ void __launch_bounds__(512, 2)
fused_s4d_mlp(const u16* __restrict__ x, const u16* __restrict__ pLogA, const u16* __restrict__ pAim, const u16* __restrict__ pCr,
              const u16* __restrict__ pCi, const u16* __restrict__ pLogdt, const u16* __restrict__ pDsk, const u16* __restrict__ w1,
              const u16* __restrict__ w2, u16* __restrict__ out, char* ws) {
    extern __shared__ __attribute__((aligned(16))) char smem_raw[];
    LAS char* lds = (LAS char*)smem_raw;
    const u32 lds0 = (u32)(uintptr_t)lds;
    unsigned long long st[20];
    const int tid = threadIdx.x, b = blockIdx.x, lane = tid & 63;
    const int wave = __builtin_amdgcn_readfirstlane(tid >> 6);
    STAMP(0);
    const int xj = b & 7, xi = b >> 3;
    const bool g1on = xi < 28;
    const int rt1 = xj * 2 + (xi >= 14 ? 1 : 0);
    const int ct = xi >= 28 ? xi - 28 : (xi >= 14 ? xi - 14 : xi);
    const int r64 = xj * 4 + (xi & 3), cd = xi >> 2;

    u32 Ev, ldtp, dskp;
    asm volatile("global_load_dword %0, %1, %2 sc1" : "=v"(Ev) : "v"(0u), "s"(ws) : "memory");
    asm volatile("s_load_dword %0, %2, %4\n\ts_load_dword %1, %3, %4"
                 : "=&s"(ldtp), "=&s"(dskp) : "s"(pLogdt), "s"(pDsk), "s"(b * 4) : "memory");
    u32x4 xt, xr, tb0; u32x2 tb1; u32 pa_, pb_, pc_, pd_, warm;
    {
        const u32 o_xt = (u32)(((b >> 1) * 16 + (tid >> 5)) * 1024 + (b & 1) * 512 + (tid & 31) * 16);
        const u32 o_xr = (u32)((b * 8 + wave) * 1024 + lane * 16);
        const u32 o_p = (u32)((b * 256 + (tid & 255)) * 2);
        const u32 o_tb = (u32)((tid & 255) * 32);
        asm volatile("global_load_dwordx4 %0, %1, %2" : "=v"(xt) : "v"(o_xt), "s"(x) : "memory");
        asm volatile("global_load_dwordx4 %0, %1, %2" : "=v"(xr) : "v"(o_xr), "s"(x) : "memory");
        asm volatile("global_load_ushort %0, %1, %2" : "=v"(pa_) : "v"(o_p), "s"(pLogA) : "memory");
        asm volatile("global_load_ushort %0, %1, %2" : "=v"(pb_) : "v"(o_p), "s"(pAim) : "memory");
        asm volatile("global_load_ushort %0, %1, %2" : "=v"(pc_) : "v"(o_p), "s"(pCr) : "memory");
        asm volatile("global_load_ushort %0, %1, %2" : "=v"(pd_) : "v"(o_p), "s"(pCi) : "memory");
        asm volatile("global_load_dwordx4 %0, %1, %2" : "=v"(tb0) : "v"(o_tb), "s"(&g_exptab) : "memory");
        asm volatile("global_load_dwordx2 %0, %1, %2 offset:16" : "=v"(tb1) : "v"(o_tb), "s"(&g_exptab) : "memory");
    }
    const u32 w1v = (u32)((lane >> 4) * 3584 + (((lane & 15) ^ ((lane >> 4) << 2)) * 16));
    const char* w1c = (const char*)w1 + ct * 256;
#pragma unroll
    for (int i = 0; i < 12; ++i) dma16(w1c + (size_t)(wave * 48 + i * 4) * 3584, w1v, lds0 + 65536 + (wave * 12 + i) * 1024);
    {
        const u32 o_w = (u32)(xi * 57344 + (wave * 56 + (lane < 56 ? lane : 55)) * 128);
        asm volatile("global_load_dword %0, %1, %2" : "=v"(warm) : "v"(o_w), "s"(w2) : "memory");
    }
    STAMP(1);
    asm volatile("s_waitcnt vmcnt(20)" : "+v"(xt) :: "memory");
    *(LAS u32x4*)(lds + L_XTILE + (tid >> 5) * 528 + (tid & 31) * 16) = xt;
    asm volatile("s_waitcnt vmcnt(19) lgkmcnt(0)" : "+v"(xr), "+v"(Ev), "+s"(ldtp), "+s"(dskp) :: "memory");
    const u32 tag = (u32)__builtin_amdgcn_readfirstlane((int)Ev) + 1u;
    {
        float v[8] = {bflo(xr.x), bfhi(xr.x), bflo(xr.y), bfhi(xr.y), bflo(xr.z), bfhi(xr.z), bflo(xr.w), bfhi(xr.w)};
        float s = ((v[0] + v[1]) + (v[2] + v[3])) + ((v[4] + v[5]) + (v[6] + v[7]));
        const float mean = wave_sum(s) * (1.f / 512.f);
        float q = 0.f;
#pragma unroll
        for (int k = 0; k < 8; ++k) { float d = v[k] - mean; q = __builtin_fmaf(d, d, q); }
        const float var = wave_sum(q) * (1.f / 512.f);
        const float den = __builtin_amdgcn_sqrtf(rbf(rbf(var) + 1e-5f));
        if (lane == 0) *(LAS u32*)(lds + L_ST + wave * 4) = pkbf(mean, den);
    }
    STAMP(2);
    BAR();
    {
        const int q = lane & 7, p = tid >> 3;
        LAS char* src = lds + L_XTILE + q * 1056 + p * 4;
        u32x4 g0 = {*(LAS u32*)(src), tag, *(LAS u32*)(src + 528), tag};
        u32x4 g1 = {*(LAS u32*)(src + 256), tag, *(LAS u32*)(src + 784), tag};
        const u32 o = WS_XT + (u32)(((b & 1) * 128 + p) * 16384 + ((b >> 1) * 16 + 2 * q) * 8);
        asm volatile("global_store_dwordx4 %0, %1, %2 sc1\n\ts_nop 1" :: "v"(o), "v"(g0), "s"(ws) : "memory");
        asm volatile("global_store_dwordx4 %0, %1, %2 sc1\n\ts_nop 1" :: "v"(o + 64u * 16384u), "v"(g1), "s"(ws) : "memory");
        if (tid < 8) {
            u32x2 g = {*(LAS u32*)(lds + L_ST + tid * 4), tag};
            asm volatile("global_store_dwordx2 %0, %1, %2 sc1" :: "v"(WS_STATS + (u32)(b * 128 + tid * 8)), "v"(g), "s"(ws) : "memory");
        }
    }
    STAMP(3);
    asm volatile("s_waitcnt vmcnt(17)" : "+v"(pa_), "+v"(pb_), "+v"(pc_), "+v"(pd_) :: "memory");
    {
        const int ch = (wave >> 1) & 1;
        const float dt = rbf(expf(ch ? bfhi(ldtp) : bflo(ldtp)));
        const float zr = -rbf(expf(bflo(pa_))) * dt, zi = bflo(pb_) * dt;
        LAS char* tp = lds + L_TAB + ch * TCH + ((wave & 1) * 64 + lane) * 4;
        if (wave < 4) {
            tp += 21 * TROW;
            const cplx w = cexp_m(zr, zi, 8.f), w2c = cmul(w, w);
            cplx e0 = {1.f, 0.f}, e1 = w;
#pragma unroll
            for (int j = 0; j < 32; j += 2) {
                *(LAS u32*)(tp + j * TROW) = pkh(e0.r, e0.i);
                *(LAS u32*)(tp + (j + 1) * TROW) = pkh(e1.r, e1.i);
                if (j < 30) { e0 = cmul(e0, w2c); e1 = cmul(e1, w2c); }
            }
        } else {
            const float cr = bflo(pc_), ci = bflo(pd_);
            const u32 eb = (__float_as_uint(wave_max(fmaxf(__builtin_fabsf(cr), __builtin_fabsf(ci)))) >> 23) & 0xffu;
            const float sc = __uint_as_float((254u - eb) << 23);
            if (lane == 0) *(LAS float*)(lds + L_SC + (wave & 3) * 4) = 2.f * dt * __uint_as_float(eb << 23);
            const cplx w1c_ = cexp_m(zr, zi, 1.f), wq = cexp_m(zr, zi, 256.f);
            cplx v[8]; v[0] = {cr * sc, ci * sc};
#pragma unroll
            for (int k = 1; k < 8; ++k) v[k] = cmul(v[k - 1], w1c_);
#pragma unroll
            for (int k = 0; k < 8; ++k) *(LAS u32*)(tp + k * TROW) = pkh(v[k].r, -v[k].i);
            cplx a0 = cmul(v[0], wq), a2 = cmul(v[2], wq), a4 = cmul(v[4], wq), a6 = cmul(v[6], wq);
            *(LAS u32*)(tp + 8 * TROW) = pkh(a0.r, -a0.i); *(LAS u32*)(tp + 9 * TROW) = pkh(a2.r, -a2.i);
            *(LAS u32*)(tp + 10 * TROW) = pkh(a4.r, -a4.i); *(LAS u32*)(tp + 11 * TROW) = pkh(a6.r, -a6.i);
            a0 = cmul(a0, wq); a4 = cmul(a4, wq);
            *(LAS u32*)(tp + 12 * TROW) = pkh(a0.r, -a0.i); *(LAS u32*)(tp + 13 * TROW) = pkh(a4.r, -a4.i);
            a0 = cmul(a0, wq); a4 = cmul(a4, wq);
            *(LAS u32*)(tp + 14 * TROW) = pkh(a0.r, -a0.i); *(LAS u32*)(tp + 15 * TROW) = pkh(a4.r, -a4.i);
#pragma unroll
            for (int k = 16; k < 21; ++k) { a0 = cmul(a0, wq); *(LAS u32*)(tp + k * TROW) = pkh(a0.r, -a0.i); }
        }
    }
    STAMP(4);
    BAR();
    {
        const int ch = wave >> 2, rtile = (wave >> 1) & 1, ctile = wave & 1, fr = lane & 15, g = lane >> 4;
        const int arow = min(16 * rtile + fr, 20);
        LAS char* pa = lds + L_TAB + ch * TCH + arow * TROW + g * 16;
        LAS char* pb = lds + L_TAB + ch * TCH + (21 + 16 * ctile + fr) * TROW + g * 16;
        f32x4 d0 = {0, 0, 0, 0}, d1 = {0, 0, 0, 0};
#pragma unroll
        for (int ks = 0; ks < 4; ++ks) {
            d0 = __builtin_amdgcn_mfma_f32_16x16x32_f16(*(LAS f16x8*)(pa + ks * 64), *(LAS f16x8*)(pb + ks * 64), d0, 0, 0, 0);
            d1 = __builtin_amdgcn_mfma_f32_16x16x32_f16(*(LAS f16x8*)(pa + 256 + ks * 64), *(LAS f16x8*)(pb + 256 + ks * 64), d1, 0, 0, 0);
        }
        const f32x2 sc = *(LAS f32x2*)(lds + L_SC + ch * 8);
        LAS char* tp = lds + L_T + ch * 3072 + (16 * rtile + 4 * g) * 144 + (16 * ctile + fr) * 4;
#pragma unroll
        for (int i = 0; i < 4; ++i)
            if (16 * rtile + 4 * g + i <= 20) *(LAS float*)(tp + i * 144) = d0[i] * sc.x + d1[i] * sc.y;
        if (tid == 0 || tid == 256) *(LAS float*)(lds + L_T + ch * 3072 + 128) = 0.f;
    }
    BAR();
    STAMP(5);
    {
        asm volatile("s_waitcnt vmcnt(0)" : "+v"(tb0), "+v"(tb1), "+v"(warm) :: "memory");
        const int ch = tid >> 8, mu = tid & 255;
        LAS char* tp = lds + L_T + ch * 3072;
        const u32 o[11] = {tb0.x & 0xffffu, tb0.x >> 16, tb0.y & 0xffffu, tb0.y >> 16, tb0.z & 0xffffu, tb0.z >> 16,
                           tb0.w & 0xffffu, tb0.w >> 16, tb1.x & 0xffffu, tb1.x >> 16, tb1.y & 0xffffu};
        float V[11];
#pragma unroll
        for (int k = 0; k < 11; ++k) V[k] = *(LAS float*)(tp + o[k]);
        u32 P[11];
#pragma unroll
        for (int k = 1; k < 11; ++k) P[k] = pkbf(V[k], V[k - 1]);
        LAS char* kp = lds + L_KC + ch * 16640 + (256 - mu) * 16;
#pragma unroll
        for (int a = 0; a < 4; ++a) { u32x4 c = {P[7 + a], P[5 + a], P[3 + a], P[1 + a]}; *(LAS u32x4*)(kp + a * 4160) = c; }
        const u32x4 z = {0, 0, 0, 0};
        if (tid < 24) *(LAS u32x4*)(lds + L_KC + (tid / 3) * 4160 + (257 + tid % 3) * 16) = z;
        if (tid >= 64 && tid < 192) { const int k = tid - 64; *(LAS u32x4*)(lds + L_UC + (k >> 5) * 4640 + (k & 31) * 16) = z; }
        if (tid >= 192 && tid < 194) { const u32x2 z2 = {0, 0}; *(LAS u32x2*)(lds + L_UC + (tid - 192) * 9280 + 512) = z2; }
    }
    STAMP(6);
    float xs[2][4], ud[2][4];
    {
        u32x4 a0, a1, s0, s1;
        const u32 ox = WS_XT + (u32)(b * 16384 + wave * 2048 + lane * 16);
        const u32 os = WS_STATS + (u32)((wave * 32 + (lane >> 2)) * 128 + (lane & 3) * 16);
        for (int spin = 0; spin < SPIN_CAP; ++spin) {
            asm volatile("global_load_dwordx4 %0, %4, %6 sc1\n\tglobal_load_dwordx4 %1, %4, %6 offset:1024 sc1\n\t"
                         "global_load_dwordx4 %2, %5, %6 sc1\n\tglobal_load_dwordx4 %3, %5, %6 offset:2048 sc1\n\ts_waitcnt vmcnt(0)"
                         : "=&v"(a0), "=&v"(a1), "=&v"(s0), "=&v"(s1) : "v"(ox), "v"(os), "s"(ws) : "memory");
            const bool ok = a0.y == tag && a0.w == tag && a1.y == tag && a1.w == tag && s0.y == tag && s0.w == tag && s1.y == tag && s1.w == tag;
            if (__all(ok)) break;
            __builtin_amdgcn_s_sleep(8);
        }
        STAMP(7);
        const u32 xv[4] = {a0.x, a0.z, a1.x, a1.z}, sv[4] = {s0.x, s0.z, s1.x, s1.z};
        const float D0 = bflo(dskp), D1 = bfhi(dskp);
        float u0[4], u1[4];
#pragma unroll
        for (int k = 0; k < 4; ++k) {
            const float m = bflo(sv[k]), ri = __builtin_amdgcn_rcpf(bfhi(sv[k]));
            xs[0][k] = bflo(xv[k]); xs[1][k] = bfhi(xv[k]);
            const u32 dm = pkbf(xs[0][k] - m, xs[1][k] - m);
            u0[k] = bflo(dm) * ri; u1[k] = bfhi(dm) * ri;
        }
        LAS char* up = lds + L_UC + (wave * 256 + 2 * lane) * 2;
#pragma unroll
        for (int gq = 0; gq < 2; ++gq) {
            const u32 c0 = pkbf(u0[2 * gq], u0[2 * gq + 1]), c1 = pkbf(u1[2 * gq], u1[2 * gq + 1]);
            *(LAS u32*)(up + gq * 256 + 520) = c0;  *(LAS u32*)(up + gq * 256 + 4640 + 512) = c0;
            *(LAS u32*)(up + gq * 256 + 9280 + 520) = c1;  *(LAS u32*)(up + gq * 256 + 9280 + 4640 + 512) = c1;
            ud[0][2 * gq] = bflo(c0) * D0; ud[0][2 * gq + 1] = bfhi(c0) * D0;
            ud[1][2 * gq] = bflo(c1) * D1; ud[1][2 * gq + 1] = bfhi(c1) * D1;
        }
    }
    BAR();
    STAMP(8);
    {
        const int ch = wave >> 2, rh = (wave >> 1) & 1, set = wave & 1, fr = lane & 15, g = lane >> 4;
        LAS char* pa = lds + L_KC + ch * 16640 + (fr & 3) * 4160 + 2 * (2048 - 8 * (fr >> 2) + 8 * g) - 3584 - rh * 256;
        LAS char* pb = lds + L_UC + ch * 9280 + (fr & 1) * 4640 + 2 * (256 + 32 * (fr >> 1) + 8 * g) - rh * 256;
        f32x4 acc[4] = {};
        if (set) conv_body<1>(pa, pb, acc); else conv_body<0>(pa, pb, acc);
        STAMP(9);
        BAR();
        const int blk = fr >> 1;
        LAS char* yp = lds + L_Y + (rh * 2 + ch) * 8192 + blk * 128 + (((2 * g + (fr & 1)) ^ (2 * (blk & 3))) * 16);
        *(LAS f32x4*)(yp + (set ? 6 : 7) * 1024) = acc[0];
        *(LAS f32x4*)(yp + (set ? 5 : 4) * 1024) = acc[1];
        *(LAS f32x4*)(yp + (set ? 2 : 3) * 1024) = acc[2];
        *(LAS f32x4*)(yp + (set ? 1 : 0) * 1024) = acc[3];
    }
#pragma unroll
    for (int i = 0; i < 4; ++i) dma16(w1c + (size_t)(384 + wave * 16 + i * 4) * 3584, w1v, lds0 + 32768 + (wave * 4 + i) * 1024);
    BAR();
    {
        const int blk = lane >> 4;
        LAS char* yp = lds + L_Y + wave * 1024 + blk * 128 + ((((lane >> 1) & 7) ^ (2 * blk)) * 16) + (lane & 1) * 8;
#pragma unroll
        for (int gq = 0; gq < 2; ++gq)
#pragma unroll
            for (int c = 0; c < 2; ++c) {
                const f32x2 p = *(LAS f32x2*)(yp + gq * 512 + c * 8192), q = *(LAS f32x2*)(yp + gq * 512 + c * 8192 + 16384);
                const u32 yb = pkbf(p.x + q.x, p.y + q.y);
                const u32 ab = pkbf(bflo(yb) + rbf(ud[c][2 * gq]), bfhi(yb) + rbf(ud[c][2 * gq + 1]));
                const u32 hb = pkbf(xs[c][2 * gq] + bflo(ab), xs[c][2 * gq + 1] + bfhi(ab));
                const u32 o = WS_H1 + (u32)(((wave * 2 + gq) * 512 + b * 2 + c) * 256 + lane * 4);
                asm volatile("global_store_dword %0, %1, %2 sc1" :: "v"(o), "v"(hb), "s"(ws) : "memory");
            }
    }
    STAMP(10);
    VMW(0);
    BAR();
    if (tid == 0) asm volatile("global_store_dword %0, %1, %2 sc1" :: "v"(WS_FLAG1 + (u32)b * 128u), "v"(tag), "s"(ws) : "memory");
    STAMP(11);
    const int fm = wave & 3, tn = wave >> 2;
    const int tq = (lane & 15) >> 2, tp_ = lane & 3, tG = (lane >> 4) & 1, th = lane >> 5;
    bf16x8 WA[32];
    f32x16 cs = {};
    {
        const u32 lo = (u32)((8 * th + tq) * 256 + (((4 * fm + 2 * tG + (tp_ >> 1)) ^ (tq << 2)) * 16) + 8 * (tp_ & 1));
#pragma unroll
        for (int i = 0; i < 32; ++i) {
            const int ks = (i & ~3) | ((i + fm) & 3);
            const u32 a = lo + (u32)ks * 4096u + (i < 24 ? 65536u : (u32)-65536);
            WA[i] = trfrag(lds + a, lds + a + 1024);
        }
        const u32x4 one4 = {0x3f803f80u, 0x3f803f80u, 0x3f803f80u, 0x3f803f80u};
        const bf16x8 ones = __builtin_bit_cast(bf16x8, one4);
        if (tn == 0) {
#pragma unroll
            for (int i = 0; i < 16; ++i) cs = __builtin_amdgcn_mfma_f32_32x32x16_bf16(WA[i], ones, cs, 0, 0, 0);
        } else {
#pragma unroll
            for (int i = 16; i < 32; ++i) cs = __builtin_amdgcn_mfma_f32_32x32x16_bf16(WA[i], ones, cs, 0, 0, 0);
        }
    }
    STAMP(12);
    if (wave == 0) {
        const u32 of = WS_FLAG1 + (u32)lane * 128u;
        for (int spin = 0; spin < SPIN_CAP; ++spin) {
            u32 f0, f1, f2, f3;
            asm volatile("global_load_dword %0, %4, %6 sc1\n\tglobal_load_dword %1, %5, %6 sc1\n\t"
                         "global_load_dword %2, %4, %7 sc1\n\tglobal_load_dword %3, %5, %7 sc1\n\ts_waitcnt vmcnt(0)"
                         : "=&v"(f0), "=&v"(f1), "=&v"(f2), "=&v"(f3) : "v"(of), "v"(of + 8192u), "s"(ws), "s"(ws + 16384) : "memory");
            if (__all(f0 == tag && f1 == tag && f2 == tag && f3 == tag)) break;
            __builtin_amdgcn_s_sleep(4);
        }
        if (b == 0 && lane == 0) asm volatile("global_store_dword %0, %1, %2 sc1" :: "v"(0u), "v"(tag), "s"(ws) : "memory");
        VMW(0);
    }
    BAR();
    STAMP(13);
    if ((lane & 31) == 0) {
#pragma unroll
        for (int k = 0; k < 4; ++k) {
            const f32x4 c4 = {cs[4 * k], cs[4 * k + 1], cs[4 * k + 2], cs[4 * k + 3]};
            *(LAS f32x4*)(lds + L_SCR + 4096 + tn * 512 + (fm * 32 + 8 * k + 4 * th) * 4) = c4;
        }
    }
    const int kq = wave >> 1, tt = wave & 1;
    u32 res[8];
    const u32 w2v = (u32)((lane >> 3) * 1024 + (((lane & 7) ^ (((lane >> 4) & 1) << 2)) * 16));
    const char* w2c = (const char*)w2 + cd * 128 + (size_t)(kq * 448 + tt * 8) * 1024;
    {
        f32x16 acc0 = {}, acc1 = {};
        float sm0 = 0.f, sq0 = 0.f, sm1 = 0.f, sq1 = 0.f;
        const char* hsrc = ws + WS_H1 + (size_t)rt1 * 131072 + wave * 2048;
        const u32 hv_ = (u32)((lane >> 4) * 256 + (((lane & 15) ^ ((lane >> 4) << 2)) * 16));
        const u32 lb = (u32)((8 * th + tq) * 256 + (((8 * tn + 2 * tG + (tp_ >> 1)) ^ (tq << 2)) * 16) + 8 * (tp_ & 1));
        const bf16x2 one2 = __builtin_bit_cast(bf16x2, 0x3f803f80u);
        if (g1on) {
#pragma unroll
            for (int c = 0; c < 4; ++c) { dma16_dev(hsrc + c * 16384, hv_, lds0 + c * 16384 + wave * 2048); dma16_dev(hsrc + c * 16384 + 1024, hv_, lds0 + c * 16384 + wave * 2048 + 1024); }
        }
#pragma unroll
        for (int c = 0; c < 8; ++c) {
            if (c <= 4) VMW(6); else if (c == 5) VMW(4 + 11); else if (c == 6) VMW(2 + 11); else VMW(11);
            BAR();
            if (c == 0) { STAMP(14); }
            if (c < 4) {
                if (g1on) { dma16_dev(hsrc + (c + 4) * 16384, hv_, lds0 + (c + 4) * 16384 + wave * 2048); dma16_dev(hsrc + (c + 4) * 16384 + 1024, hv_, lds0 + (c + 4) * 16384 + wave * 2048 + 1024); }
            }
            if (c == 4) {
                const u32 orr = WS_H1 + (u32)(((r64 >> 1) * 512 + cd * 64 + kq * 16 + 4 * th) * 256 + ((r64 & 1) * 64 + tt * 32 + (lane & 31)) * 2);
#pragma unroll
                for (int k = 0; k < 8; ++k)
                    asm volatile("global_load_ushort %0, %1, %2 offset:%3 sc1" : "=v"(res[k]) : "v"(orr), "s"(ws), "i"((k & 3) * 256 + (k >> 2) * 2048) : "memory");
#pragma unroll
                for (int s = 0; s < 3; ++s) dma16(w2c + s * 16384, w2v, lds0 + 131072 + s * 8192 + wave * 1024);
            }
            if (g1on) {
#pragma unroll
                for (int r = 0; r < 4; ++r) {
                    const int i = 4 * c + r;
                    const u32 a = lb + (u32)(4 * c + ((r + fm) & 3)) * 4096u;
                    const bf16x8 B0 = trfrag(lds + a, lds + a + 1024), B1 = trfrag(lds + (a ^ 64u), lds + (a ^ 64u) + 1024);
                    acc0 = __builtin_amdgcn_mfma_f32_32x32x16_bf16(WA[i], B0, acc0, 0, 0, 0);
                    acc1 = __builtin_amdgcn_mfma_f32_32x32x16_bf16(WA[i], B1, acc1, 0, 0, 0);
                    if (r == 0) {
                        const u32x4 q0 = __builtin_bit_cast(u32x4, B0), q1 = __builtin_bit_cast(u32x4, B1);
#pragma unroll
                        for (int k = 0; k < 4; ++k) {
                            const u32 d0 = q0[k], d1 = q1[k];
                            const bf16x2 e0 = __builtin_bit_cast(bf16x2, d0), e1 = __builtin_bit_cast(bf16x2, d1);
                            sm0 = __builtin_amdgcn_fdot2_f32_bf16(e0, one2, sm0, false); sq0 = __builtin_amdgcn_fdot2_f32_bf16(e0, e0, sq0, false);
                            sm1 = __builtin_amdgcn_fdot2_f32_bf16(e1, one2, sm1, false); sq1 = __builtin_amdgcn_fdot2_f32_bf16(e1, e1, sq1, false);
                        }
                    }
                }
            }
        }
        STAMP(15);
        {
            auto x0 = __builtin_amdgcn_permlane32_swap(__float_as_uint(sm0), __float_as_uint(sm1), false, false);
            auto x1 = __builtin_amdgcn_permlane32_swap(__float_as_uint(sq0), __float_as_uint(sq1), false, false);
            const f32x2 pr = {__uint_as_float(x0[0]) + __uint_as_float(x0[1]), __uint_as_float(x1[0]) + __uint_as_float(x1[1])};
            *(LAS f32x2*)(lds + L_SCR + (tn * 4 + fm) * 512 + lane * 8) = pr;
        }
        BAR();
        if (g1on) {
            float mu_[2], ri_[2];
#pragma unroll
            for (int u = 0; u < 2; ++u) {
                LAS char* sp = lds + L_SCR + tn * 2048 + (u * 32 + (lane & 31)) * 8;
                const f32x2 p0 = *(LAS f32x2*)(sp), p1 = *(LAS f32x2*)(sp + 512), p2 = *(LAS f32x2*)(sp + 1024), p3 = *(LAS f32x2*)(sp + 1536);
                const float mean = ((p0.x + p1.x) + (p2.x + p3.x)) * (1.f / 512.f);
                const float var = fmaxf(((p0.y + p1.y) + (p2.y + p3.y)) * (1.f / 512.f) - mean * mean, 0.f);
                mu_[u] = rbf(mean);
                ri_[u] = __builtin_amdgcn_rcpf(rbf(__builtin_amdgcn_sqrtf(rbf(rbf(var) + 1e-5f))));
            }
            float cf[16];
#pragma unroll
            for (int k = 0; k < 4; ++k) {
                LAS char* cp = lds + L_SCR + 4096 + (fm * 32 + 8 * k + 4 * th) * 4;
                const f32x4 c0 = *(LAS f32x4*)(cp), c1 = *(LAS f32x4*)(cp + 512);
#pragma unroll
                for (int e = 0; e < 4; ++e) cf[4 * k + e] = c0[e] + c1[e];
            }
#pragma unroll
            for (int u = 0; u < 2; ++u)
#pragma unroll
                for (int s = 0; s < 2; ++s) {
                    u32x4 hq;
#pragma unroll
                    for (int k = 0; k < 4; ++k) {
                        const int r = 8 * s + 2 * k;
                        const float a0 = (u ? acc1[r] : acc0[r]), a1 = (u ? acc1[r + 1] : acc0[r + 1]);
                        hq[k] = pkbf(fmaxf((a0 - mu_[u] * cf[r]) * ri_[u], 0.f), fmaxf((a1 - mu_[u] * cf[r + 1]) * ri_[u], 0.f));
                    }
                    const int ks = ct * 8 + fm * 2 + s, q = ks / 28;
                    const char* dst = ws + WS_HH + (size_t)((((rt1 * 2 + tn) * 4 + q) * 2 + u) * 28 + (ks - q * 28)) * 1024;
                    asm volatile("global_store_dwordx4 %0, %1, %2 sc1\n\ts_nop 1" :: "v"((u32)lane * 16u), "v"(hq), "s"(dst) : "memory");
                }
        }
    }
#pragma unroll
    for (int s = 3; s < 19; ++s) dma16(w2c + s * 16384, w2v, lds0 + (s - 3) * 8192 + wave * 1024);
    VMW(16);
    BAR();
    if (g1on && tid == 0) asm volatile("global_store_dword %0, %1, %2 sc1" :: "v"(WS_FLAG2 + (u32)(rt1 * 14 + ct) * 128u), "v"(tag), "s"(ws) : "memory");
    STAMP(16);
    const u32 la = (u32)(kq * 2048 + (4 * th + tq) * 128 + (((2 * tG + (tp_ >> 1)) ^ ((tq >> 1) << 2)) * 16) + 8 * (tp_ & 1));
    bf16x8 WB[9][2];
    VMW(10);
    BAR();
#pragma unroll
    for (int i = 0; i < 9; ++i) {
        const u32 a = la + (i < 3 ? 131072u + i * 8192u : (i - 3) * 8192u);
        WB[i][0] = trfrag(lds + a, lds + a + 1024); WB[i][1] = trfrag(lds + (a ^ 64u), lds + (a ^ 64u) + 1024);
    }
    BAR();
#pragma unroll
    for (int s = 19; s < 28; ++s) dma16(w2c + s * 16384, w2v, lds0 + (s - 19 < 3 ? 131072 + (s - 19) * 8192 : (s - 22) * 8192) + wave * 1024);
    if (wave == 0) {
        const u32 of = WS_FLAG2 + (u32)((r64 >> 1) * 14 + (lane < 14 ? lane : 0)) * 128u;
        for (int spin = 0; spin < SPIN_CAP; ++spin) {
            u32 f0;
            asm volatile("global_load_dword %0, %1, %2 sc1\n\ts_waitcnt vmcnt(0)" : "=&v"(f0) : "v"(of), "s"(ws) : "memory");
            if (__all(f0 == tag)) break;
            __builtin_amdgcn_s_sleep(4);
        }
    }
    asm volatile("s_waitcnt vmcnt(0)" : "+v"(res[0]), "+v"(res[1]), "+v"(res[2]), "+v"(res[3]), "+v"(res[4]), "+v"(res[5]), "+v"(res[6]), "+v"(res[7]) :: "memory");
    BAR();
    STAMP(17);
    f32x16 o0 = {}, o1 = {};
    {
        constexpr int PF = 12;
        const char* hsrc = ws + WS_HH + (size_t)((r64 * 4 + kq) * 2 + tt) * 28672;
        const u32 vo = (u32)lane * 16u;
        u32x4 hv[28];
#pragma unroll
        for (int i = 0; i < PF; ++i) asm volatile("global_load_dwordx4 %0, %1, %2 offset:%3 sc1" : "=v"(hv[i]) : "v"(vo), "s"(hsrc + (i >> 2) * 4096), "i"((i & 3) * 1024) : "memory");
#pragma unroll
        for (int i = 0; i < 28; ++i) {
            asm volatile("s_waitcnt vmcnt(%1)" : "+v"(hv[i]) : "i"(i + PF <= 28 ? PF - 1 : 27 - i) : "memory");
            if (i + PF < 28) asm volatile("global_load_dwordx4 %0, %1, %2 offset:%3 sc1" : "=v"(hv[i + PF]) : "v"(vo), "s"(hsrc + ((i + PF) >> 2) * 4096), "i"(((i + PF) & 3) * 1024) : "memory");
            bf16x8 A0, A1;
            if (i < 9) { A0 = WB[i][0]; A1 = WB[i][1]; }
            else {
                const u32 a = la + (i < 19 ? (i - 3) * 8192u : (i - 19 < 3 ? 131072u + (i - 19) * 8192u : (i - 22) * 8192u));
                A0 = trfrag(lds + a, lds + a + 1024); A1 = trfrag(lds + (a ^ 64u), lds + (a ^ 64u) + 1024);
            }
            const bf16x8 Bf = __builtin_bit_cast(bf16x8, hv[i]);
            o0 = __builtin_amdgcn_mfma_f32_32x32x16_bf16(A0, Bf, o0, 0, 0, 0);
            o1 = __builtin_amdgcn_mfma_f32_32x32x16_bf16(A1, Bf, o1, 0, 0, 0);
        }
    }
    STAMP(18);
    BAR();
    {
        LAS char* xp = lds + tt * 32768 + kq * 2048 + lane * 16;
#pragma unroll
        for (int dq = 0; dq < 4; ++dq) {
            const f32x16& o = (dq >> 1) ? o1 : o0;
            const int r = 8 * (dq & 1);
            const f32x4 v0 = {o[r], o[r + 1], o[r + 2], o[r + 3]}, v1 = {o[r + 4], o[r + 5], o[r + 6], o[r + 7]};
            *(LAS f32x4*)(xp + dq * 8192) = v0; *(LAS f32x4*)(xp + dq * 8192 + 1024) = v1;
        }
        BAR();
        LAS char* rp = lds + tt * 32768 + kq * 8192 + lane * 16;
        f32x4 s0 = *(LAS f32x4*)(rp), s1 = *(LAS f32x4*)(rp + 1024);
#pragma unroll
        for (int s = 1; s < 4; ++s) { s0 += *(LAS f32x4*)(rp + s * 2048); s1 += *(LAS f32x4*)(rp + s * 2048 + 1024); }
        u32 p0 = pkbf(s0[0], s0[1]), p1 = pkbf(s0[2], s0[3]), p2 = pkbf(s1[0], s1[1]), p3 = pkbf(s1[2], s1[3]);
        p0 = pkbf(bflo(p0) + bflo(res[0]), bfhi(p0) + bflo(res[1])); p1 = pkbf(bflo(p1) + bflo(res[2]), bfhi(p1) + bflo(res[3]));
        p2 = pkbf(bflo(p2) + bflo(res[4]), bfhi(p2) + bflo(res[5])); p3 = pkbf(bflo(p3) + bflo(res[6]), bfhi(p3) + bflo(res[7]));
        auto y0 = __builtin_amdgcn_permlane32_swap(p0, p2, false, false);
        auto y1 = __builtin_amdgcn_permlane32_swap(p1, p3, false, false);
        const u32x4 ov = {y0[0], y1[0], y0[1], y1[1]};
        *(u32x4*)(out + (size_t)(r64 * 64 + tt * 32 + (lane & 31)) * 512 + cd * 64 + kq * 16 + th * 8) = ov;
    }
    STAMP(19);
    { const unsigned long long lim = st[0] + 6000ull + 10ull * (st[0] - st[0]); while (__builtin_amdgcn_s_memrealtime() < lim) __builtin_amdgcn_s_sleep(2); }
}

extern "C" void kernel_launch(void* const* d_in, const int* in_sizes, int n_in, void* d_out, int out_size, void* d_ws, size_t ws_size,
                              hipStream_t stream) {
    (void)hipFuncSetAttribute((const void*)fused_s4d_mlp, hipFuncAttributeMaxDynamicSharedMemorySize, LDS_BYTES);
    fused_s4d_mlp<<<256, 512, LDS_BYTES, stream>>>((const u16*)d_in[0], (const u16*)d_in[1], (const u16*)d_in[2], (const u16*)d_in[3],
                                                    (const u16*)d_in[4], (const u16*)d_in[5], (const u16*)d_in[6], (const u16*)d_in[8],
                                                    (const u16*)d_in[9], (u16*)d_out, (char*)d_ws);
}
```

```cpp
#include <hip/hip_runtime.h>
#include <stdint.h>

typedef unsigned short u16;
typedef unsigned u32;
typedef u32 u32x4 __attribute__((ext_vector_type(4)));
typedef u32 u32x2 __attribute__((ext_vector_type(2)));
typedef float f32x2 __attribute__((ext_vector_type(2)));
typedef float f32x4 __attribute__((ext_vector_type(4)));
typedef float f32x16 __attribute__((ext_vector_type(16)));
typedef __bf16 bf16x2 __attribute__((ext_vector_type(2)));
typedef __bf16 bf16x8 __attribute__((ext_vector_type(8)));
typedef _Float16 f16x2 __attribute__((ext_vector_type(2)));
typedef _Float16 f16x8 __attribute__((ext_vector_type(8)));
typedef short s16x2 __attribute__((ext_vector_type(2)));
typedef short s16x4 __attribute__((ext_vector_type(4)));
#define LAS __attribute__((address_space(3)))

constexpr u32 WS_EPOCH = 0;
constexpr u32 WS_STATS = 4096;
constexpr u32 WS_FLAG1 = 40960;
constexpr u32 WS_ST2   = 110592;
constexpr u32 WS_CS    = 131072;
constexpr u32 WS_FLAG2 = 147456;
constexpr u32 WS_H1    = 147456 + 458752;
constexpr u32 WS_XT    = WS_H1 + (2u << 20);
constexpr u32 WS_HH    = WS_XT + (4u << 20);
constexpr u32 WS_STAMP = 200u << 20;

constexpr u32 L_TAB   = 0;
constexpr u32 TROW = 544, TCH = 53 * 544;
constexpr u32 L_KC    = 0;
constexpr u32 L_UC    = 33280;
constexpr u32 L_XTILE = 65536;
constexpr u32 L_T     = 57664;
constexpr u32 L_SC    = 64576;
constexpr u32 L_Y     = 0;
constexpr u32 L_SCR   = 155648;
constexpr u32 LDS_BYTES = 163840;

struct ExpTab { u16 v[256 * 16]; };
constexpr ExpTab make_exptab() {
    ExpTab t{};
    for (int mu = 0; mu < 256; ++mu)
        for (int k = 0; k < 16; ++k) {
            int l = 8 * mu + k - 3, off = 0;
            if (k < 11 && l >= 0) {
                int s = (l >= 1024) + (l >= 512) + (l >= 256);
                int pos = s ? (((l + (1 << (s - 1)) - 1 + ((l >> s) & 1)) >> s) << s) : l;
                int i = pos >> 8, col = (pos >> 3) & 31, bb = pos & 7;
                int row = i == 0 ? bb : i == 1 ? 8 + bb / 2 : i < 4 ? 12 + 2 * (i - 2) + bb / 4 : 12 + i;
                off = row * 144 + col * 4;
            }
            t.v[mu * 16 + k] = (u16)off;
        }
    return t;
}
static __device__ const ExpTab g_exptab = make_exptab();

#define BAR() asm volatile("s_waitcnt lgkmcnt(0)\n\ts_barrier" ::: "memory")
#define VMW(n) asm volatile("s_waitcnt vmcnt(%0)" :: "i"(n) : "memory")
#define STAMP(k)

__device__ __forceinline__ float bflo(u32 v) { return __uint_as_float(v << 16); }
__device__ __forceinline__ float bfhi(u32 v) { return __uint_as_float(v & 0xffff0000u); }
__device__ __forceinline__ u32 pkbf(float a, float b) { f32x2 x = {a, b}; bf16x2 r = __builtin_convertvector(x, bf16x2); return __builtin_bit_cast(u32, r); }
__device__ __forceinline__ float rbf(float a) { return bflo(pkbf(a, a)); }
template <int CTRL> __device__ __forceinline__ float dpp_add(float v) {
    return v + __int_as_float(__builtin_amdgcn_update_dpp(0, __float_as_int(v), CTRL, 0xf, 0xf, true));
}
template <int CTRL> __device__ __forceinline__ float dpp_max(float v) {
    return fmaxf(v, __int_as_float(__builtin_amdgcn_update_dpp(0, __float_as_int(v), CTRL, 0xf, 0xf, true)));
}
__device__ __forceinline__ float wave_sum(float v) {
    v = dpp_add<0xb1>(v); v = dpp_add<0x4e>(v); v = dpp_add<0x141>(v); v = dpp_add<0x140>(v);
    int i = __float_as_int(v);
    return (__int_as_float(__builtin_amdgcn_readlane(i, 0)) + __int_as_float(__builtin_amdgcn_readlane(i, 16))) +
           (__int_as_float(__builtin_amdgcn_readlane(i, 32)) + __int_as_float(__builtin_amdgcn_readlane(i, 48)));
}
__device__ __forceinline__ float wave_max(float v) {
    v = dpp_max<0xb1>(v); v = dpp_max<0x4e>(v); v = dpp_max<0x141>(v); v = dpp_max<0x140>(v);
    int i = __float_as_int(v);
    return fmaxf(fmaxf(__int_as_float(__builtin_amdgcn_readlane(i, 0)), __int_as_float(__builtin_amdgcn_readlane(i, 16))),
                 fmaxf(__int_as_float(__builtin_amdgcn_readlane(i, 32)), __int_as_float(__builtin_amdgcn_readlane(i, 48))));
}
__device__ __forceinline__ void dma16(const void* sbase, u32 voff, u32 lds) {
    asm volatile("s_mov_b32 m0, %2\n\ts_nop 0\n\tglobal_load_lds_dwordx4 %0, %1" :: "v"(voff), "s"(sbase), "s"(lds) : "memory");
}
__device__ __forceinline__ void dma16_dev(const void* sbase, u32 voff, u32 lds) {
    asm volatile("s_mov_b32 m0, %2\n\ts_nop 0\n\tglobal_load_lds_dwordx4 %0, %1 sc1" :: "v"(voff), "s"(sbase), "s"(lds) : "memory");
}
typedef f32x2 cplx;
__device__ __forceinline__ void cmul2(cplx& ra, cplx& rb, cplx a, cplx b, cplx w) {
    asm("v_pk_mul_f32 %0, %2, %4 op_sel_hi:[1,0]\n\t"
        "v_pk_mul_f32 %1, %3, %4 op_sel_hi:[1,0]\n\t"
        "v_pk_fma_f32 %0, %2, %4, %0 op_sel:[1,1,0] op_sel_hi:[0,1,1] neg_lo:[1,0,0]\n\t"
        "v_pk_fma_f32 %1, %3, %4, %1 op_sel:[1,1,0] op_sel_hi:[0,1,1] neg_lo:[1,0,0]"
        : "=&v"(ra), "=&v"(rb) : "v"(a), "v"(b), "v"(w));
}
__device__ __forceinline__ cplx cmul(cplx a, cplx w) {
    cplx r;
    asm("v_pk_mul_f32 %0, %1, %2 op_sel_hi:[1,0]\n\ts_nop 0\n\t"
        "v_pk_fma_f32 %0, %1, %2, %0 op_sel:[1,1,0] op_sel_hi:[0,1,1] neg_lo:[1,0,0]"
        : "=&v"(r) : "v"(a), "v"(w));
    return r;
}
__device__ __forceinline__ u32 pkh(cplx x) { f16x2 r = __builtin_convertvector(x, f16x2); return __builtin_bit_cast(u32, r); }
#define TOUCH 1
#define PFD 12
#define STAMPA(k) STAMP(k)
#define STAMPB(k)
#define AFIN 0
#define LKS 0
#define SLP2 4
#define T2L 22
#define W1R 2
#define BDD 4
#define SCHED() __builtin_amdgcn_sched_barrier(0)
__device__ __forceinline__ float exp_c(float x) {
    const float t = x * 1.44269504088896341f;
    const float r = __builtin_fmaf(x, 1.44269504088896341f, -t) + x * 1.92596299e-8f;
    const float e = __builtin_amdgcn_exp2f(t);
    return __builtin_fmaf(e * r, 0.693147180559945f, e);
}
__device__ __forceinline__ cplx cexp_m(float zr, float zi, float m) {
    float mag = __builtin_amdgcn_exp2f(zr * m * 1.44269504088896341f);
    float p = zi * m;
    float k = __builtin_rintf(p * 0.15915494309189535f);
    float f = __builtin_fmaf(p, 0.15915494309189535f, -k) + p * 6.4206383e-9f;
    return cplx{mag * __builtin_amdgcn_cosf(f), mag * __builtin_amdgcn_sinf(f)};
}
__device__ __forceinline__ bf16x8 trfrag(LAS char* p0, LAS char* p1) {
    s16x4 a = __builtin_amdgcn_ds_read_tr16_b64_v4i16((LAS s16x4*)p0);
    s16x4 b = __builtin_amdgcn_ds_read_tr16_b64_v4i16((LAS s16x4*)p1);
    u32x2 ua = __builtin_bit_cast(u32x2, a), ub = __builtin_bit_cast(u32x2, b);
    u32x4 r = {ua.x, ua.y, ub.x, ub.y};
    return __builtin_bit_cast(bf16x8, r);
}
constexpr int SPIN_CAP = 1 << 15;

template <int SET> __device__ __forceinline__ void conv_body(LAS char* pa, LAS char* pb, f32x4 (&acc)[4], bool dma, const char* wsrc, u32 wv, u32 wdst) {
    constexpr int T0 = SET ? 6 : 7, T1 = SET ? 5 : 4, T2 = SET ? 2 : 3, T3 = SET ? 1 : 0;
    constexpr int NF = T0 + 1;
    bf16x8 A[2][NF], B[2][NF];
#pragma unroll
    for (int a = 0; a < NF; ++a) { A[0][a] = *(LAS bf16x8*)(pa + 192 + 512 * (7 - a)); B[0][T0 - a] = *(LAS bf16x8*)(pb + 192 + 512 * (T0 - a)); }
    SCHED();
#pragma unroll
    for (int r = 0; r < 4; ++r) {
        const int cu = r & 1, nx = cu ^ 1;
#pragma unroll
        for (int a = 0; a < NF; ++a) {
            if (r < 3) { A[nx][a] = *(LAS bf16x8*)(pa + 128 - 64 * r + 512 * (7 - a)); B[nx][T0 - a] = *(LAS bf16x8*)(pb + 128 - 64 * r + 512 * (T0 - a)); }
            if (W1R == 0 && (a == 1 || a == 3 || a == 5)) { if (dma) dma16(wsrc + (size_t)(r * 12 + (a >> 1) * 4) * 3584, wv, wdst + (r * 3 + (a >> 1)) * 1024); }
            if (W1R == 1 && r >= 1 && a >= 2 && a <= 5) { if (dma) dma16(wsrc + (size_t)((r - 1) * 16 + (a - 2) * 4) * 3584, wv, wdst + ((r - 1) * 4 + a - 2) * 1024); }
            if (W1R == 2 && r >= 2 && a >= 1 && a <= 6) { if (dma) dma16(wsrc + (size_t)((r - 2) * 24 + (a - 1) * 4) * 3584, wv, wdst + ((r - 2) * 6 + a - 1) * 1024); }
            acc[0] = __builtin_amdgcn_mfma_f32_16x16x32_bf16(A[cu][a], B[cu][T0 - a], acc[0], 0, 0, 0);
            if (a <= T1) acc[1] = __builtin_amdgcn_mfma_f32_16x16x32_bf16(A[cu][a], B[cu][T1 - a], acc[1], 0, 0, 0);
            if (a <= T2) acc[2] = __builtin_amdgcn_mfma_f32_16x16x32_bf16(A[cu][a], B[cu][T2 - a], acc[2], 0, 0, 0);
            if (a <= T3) acc[3] = __builtin_amdgcn_mfma_f32_16x16x32_bf16(A[cu][a], B[cu][T3 - a], acc[3], 0, 0, 0);
            SCHED();
        }
    }
}

__global__ void __launch_bounds__(512, 2)
fused_s4d_mlp(const u16* __restrict__ x, const u16* __restrict__ pLogA, const u16* __restrict__ pAim, const u16* __restrict__ pCr,
              const u16* __restrict__ pCi, const u16* __restrict__ pLogdt, const u16* __restrict__ pDsk, const u16* __restrict__ w1,
              const u16* __restrict__ w2, u16* __restrict__ out, char* ws) {
    extern __shared__ __attribute__((aligned(16))) char smem_raw[];
    LAS char* lds = (LAS char*)smem_raw;
    const u32 lds0 = (u32)(uintptr_t)lds;
    const int tid = threadIdx.x, b = blockIdx.x, lane = tid & 63;
    const int wave = __builtin_amdgcn_readfirstlane(tid >> 6);
    STAMP(0);
    const int xj = b & 7, xi = b >> 3;
    const bool g1on = xi < 28;
    const int rt1 = (xj >> 1) * 4 + (xi & 3);
    const int ct = (xj & 1) * 7 + (g1on ? xi >> 2 : 0);
    asm volatile("" :: "s"(out), "s"(w1), "s"(w2));
    const int r64 = xj * 4 + (xi & 3), cd = xi >> 2;

    u32 Ev, ldtp, dskp;
    u32x4 xt, xr, tb0; u32x2 tb1; u32 pa_, pb_, pc_, pd_, warm;
    {
        const u32 o_xt = (u32)(((b >> 1) * 16 + (lane >> 2)) * 1024 + (b & 1) * 512 + wave * 64 + (lane & 3) * 16);
        const u32 o_xr = (u32)((b * 8 + wave) * 1024 + lane * 16);
        const u32 o_p = (u32)((b * 256 + (tid & 255)) * 2);
        const u32 o_tb = (u32)((tid & 255) * 32);
        asm volatile("global_load_dwordx4 %0, %1, %2" : "=v"(xt) : "v"(o_xt), "s"(x) : "memory");
        asm volatile("global_load_dword %0, %1, %2 sc1" : "=v"(Ev) : "v"(0u), "s"(ws) : "memory");
        asm volatile("global_load_dwordx4 %0, %1, %2" : "=v"(xr) : "v"(o_xr), "s"(x) : "memory");
        asm volatile("s_load_dword %0, %2, %4\n\ts_load_dword %1, %3, %4"
                     : "=&s"(ldtp), "=&s"(dskp) : "s"(pLogdt), "s"(pDsk), "s"(b * 4) : "memory");
        asm volatile("global_load_ushort %0, %1, %2" : "=v"(pa_) : "v"(o_p), "s"(pLogA) : "memory");
        asm volatile("global_load_ushort %0, %1, %2" : "=v"(pb_) : "v"(o_p), "s"(pAim) : "memory");
        asm volatile("global_load_ushort %0, %1, %2" : "=v"(pc_) : "v"(o_p), "s"(pCr) : "memory");
        asm volatile("global_load_ushort %0, %1, %2" : "=v"(pd_) : "v"(o_p), "s"(pCi) : "memory");
        asm volatile("global_load_dwordx4 %0, %1, %2" : "=v"(tb0) : "v"(o_tb), "s"(&g_exptab) : "memory");
        asm volatile("global_load_dwordx2 %0, %1, %2 offset:16" : "=v"(tb1) : "v"(o_tb), "s"(&g_exptab) : "memory");
    }
    const u32 w1v = (u32)((lane >> 4) * 3584 + (((lane & 15) ^ ((lane >> 4) << 2)) * 16));
    const char* w1c = (const char*)w1 + ct * 256;
    u32 zaddr;
    {
        const int k = tid - 2;
        zaddr = tid < 2 ? L_UC + tid * 9280 + 512 : k < 128 ? L_UC + (k >> 5) * 4640 + (k & 31) * 16 : L_KC + ((k - 128) & 7) * 4160 + (257 + ((k - 128) >> 3)) * 16;
    }
    STAMPA(1);
    asm volatile("s_waitcnt vmcnt(7)" : "+v"(xt), "+v"(Ev) :: "memory");
    const u32 tag = (u32)__builtin_amdgcn_readfirstlane((int)Ev) + 1u;
    {
        LAS char* tl = lds + L_XTILE + wave * 1280;
        *(LAS u32x4*)(tl + (lane >> 2) * 80 + (lane & 3) * 16) = xt;
        const int q = lane & 7, p = lane >> 3;
        LAS char* src = tl + q * 160 + p * 4;
        u32x4 g0 = {*(LAS u32*)(src), tag, *(LAS u32*)(src + 80), tag};
        u32x4 g1 = {*(LAS u32*)(src + 32), tag, *(LAS u32*)(src + 112), tag};
        const u32 o = WS_XT + (u32)(((b & 1) * 128 + wave * 16 + p) * 16384 + ((b >> 1) * 16 + 2 * q) * 8);
        asm volatile("global_store_dwordx4 %0, %2, %4 sc1\n\tglobal_store_dwordx4 %1, %3, %4 sc1\n\ts_nop 1" :: "v"(o), "v"(o + 8u * 16384u), "v"(g0), "v"(g1), "s"(ws) : "memory");
    }
    STAMPA(2);
    asm volatile("s_waitcnt vmcnt(8)" : "+v"(xr) :: "memory");
    {
        float v[8] = {bflo(xr.x), bfhi(xr.x), bflo(xr.y), bfhi(xr.y), bflo(xr.z), bfhi(xr.z), bflo(xr.w), bfhi(xr.w)};
        float s = ((v[0] + v[1]) + (v[2] + v[3])) + ((v[4] + v[5]) + (v[6] + v[7]));
        const float mean = wave_sum(s) * (1.f / 512.f);
        float q = 0.f;
#pragma unroll
        for (int k = 0; k < 8; ++k) { float d = v[k] - mean; q = __builtin_fmaf(d, d, q); }
        const float var = wave_sum(q) * (1.f / 512.f);
        const float den = __builtin_amdgcn_sqrtf(rbf(rbf(var) + 1e-5f));
        const u32x2 g = {pkbf(mean, den), tag};
        if (lane == 0) asm volatile("global_store_dwordx2 %0, %1, %2 sc1" :: "v"(WS_STATS + (u32)(b * 128 + wave * 8)), "v"(g), "s"(ws) : "memory");
    }
    STAMPA(3);
    asm volatile("s_waitcnt vmcnt(5) lgkmcnt(0)" : "+v"(pa_), "+v"(pb_), "+v"(pc_), "+v"(pd_), "+s"(ldtp), "+s"(dskp) :: "memory");
    if (TOUCH == 0) {
        const u32 o_w = (u32)(b * 7168 + (lane < 56 ? lane : 55) * 128);
        asm volatile("global_load_dword %0, %1, %2" : "=v"(warm) : "v"(wave < 2 ? o_w : 0u), "s"(wave == 0 ? w1 : w2) : "memory");
    } else {
        const int wl = lane < 28 ? lane : 27;
        const u32 o_w = (u32)((xi * 16 + wave * 2 + (wl >= 14 ? 1 : 0)) * 3584 + (xj & 1) * 1792 + (wl >= 14 ? wl - 14 : wl) * 128);
        const u32 o_v = (u32)(b * 7168 + wave * 896 + (lane - 28 < 7 ? lane - 28 : 6) * 128);
        asm volatile("global_load_dword %0, %1, off" : "=v"(warm) : "v"(lane < 28 ? (const char*)w1 + o_w : (const char*)w2 + o_v) : "memory");
    }
    {
        const int ch = (wave >> 1) & 1;
        const float dt = rbf(exp_c(ch ? bfhi(ldtp) : bflo(ldtp)));
        const float zr = -rbf(exp_c(bflo(pa_))) * dt, zi = bflo(pb_) * dt;
        LAS char* tp = lds + L_TAB + ch * TCH + ((wave & 1) * 64 + lane) * 4;
        if (wave < 4) {
            tp += 21 * TROW;
            cplx e0 = {1.f, 0.f}, e1 = cexp_m(zr, zi, 8.f), e2 = cmul(e1, e1), e3, w4;
            cmul2(e3, w4, e1, e2, e2);
#pragma unroll
            for (int j = 0; j < 32; j += 4) {
                *(LAS u32*)(tp + j * TROW) = pkh(e0); *(LAS u32*)(tp + (j + 1) * TROW) = pkh(e1);
                *(LAS u32*)(tp + (j + 2) * TROW) = pkh(e2); *(LAS u32*)(tp + (j + 3) * TROW) = pkh(e3);
                if (j < 28) { cplx n0, n1, n2, n3; cmul2(n0, n1, e0, e1, w4); cmul2(n2, n3, e2, e3, w4); e0 = n0; e1 = n1; e2 = n2; e3 = n3; }
            }
        } else {
            const float cr = bflo(pc_), ci = bflo(pd_);
            const u32 eb = (__float_as_uint(wave_max(fmaxf(__builtin_fabsf(cr), __builtin_fabsf(ci)))) >> 23) & 0xffu;
            const float sc = __uint_as_float((254u - eb) << 23);
            if (lane == 0) *(LAS float*)(lds + L_SC + (wave & 3) * 4) = 2.f * dt * __uint_as_float(eb << 23);
            const cplx wa = cexp_m(zr, -zi, 1.f), wq = cexp_m(zr, -zi, 256.f), wb = cmul(wa, wa);
            cplx v0 = {cr * sc, -ci * sc}, v1 = cmul(v0, wa), v2, v3, v4, v5, v6, v7, a0, a2, a4, a6;
            cmul2(v2, v3, v0, v1, wb); cmul2(v4, v5, v2, v3, wb); cmul2(v6, v7, v4, v5, wb);
            *(LAS u32*)(tp + 0 * TROW) = pkh(v0); *(LAS u32*)(tp + 1 * TROW) = pkh(v1); *(LAS u32*)(tp + 2 * TROW) = pkh(v2); *(LAS u32*)(tp + 3 * TROW) = pkh(v3);
            *(LAS u32*)(tp + 4 * TROW) = pkh(v4); *(LAS u32*)(tp + 5 * TROW) = pkh(v5); *(LAS u32*)(tp + 6 * TROW) = pkh(v6); *(LAS u32*)(tp + 7 * TROW) = pkh(v7);
            cmul2(a0, a4, v0, v4, wq); cmul2(a2, a6, v2, v6, wq);
            *(LAS u32*)(tp + 8 * TROW) = pkh(a0); *(LAS u32*)(tp + 9 * TROW) = pkh(a2); *(LAS u32*)(tp + 10 * TROW) = pkh(a4); *(LAS u32*)(tp + 11 * TROW) = pkh(a6);
            cmul2(v0, v4, a0, a4, wq);
            *(LAS u32*)(tp + 12 * TROW) = pkh(v0); *(LAS u32*)(tp + 13 * TROW) = pkh(v4);
            cmul2(a0, a4, v0, v4, wq);
            *(LAS u32*)(tp + 14 * TROW) = pkh(a0); *(LAS u32*)(tp + 15 * TROW) = pkh(a4);
            const cplx wq2 = cmul(wq, wq);
            v1 = cmul(a0, wq);
            cmul2(v2, v3, a0, v1, wq2);
            cmul2(v4, v5, v2, v3, wq2);
            *(LAS u32*)(tp + 16 * TROW) = pkh(v1); *(LAS u32*)(tp + 17 * TROW) = pkh(v2); *(LAS u32*)(tp + 18 * TROW) = pkh(v3);
            *(LAS u32*)(tp + 19 * TROW) = pkh(v4); *(LAS u32*)(tp + 20 * TROW) = pkh(v5);
        }
    }
    STAMPA(4);
    BAR();
    {
        const int ch = wave >> 2, rtile = (wave >> 1) & 1, ctile = wave & 1, fr = lane & 15, g = lane >> 4;
        const int arow = min(16 * rtile + fr, 20);
        LAS char* pa = lds + L_TAB + ch * TCH + arow * TROW + g * 16;
        LAS char* pb = lds + L_TAB + ch * TCH + (21 + 16 * ctile + fr) * TROW + g * 16;
        f32x4 d0 = {0, 0, 0, 0}, d1 = {0, 0, 0, 0};
        f16x8 fa[8], fb[8];
#pragma unroll
        for (int ks = 0; ks < 8; ++ks) { fa[ks] = *(LAS f16x8*)(pa + ks * 64); fb[ks] = *(LAS f16x8*)(pb + ks * 64); }
        const f32x2 sc = *(LAS f32x2*)(lds + L_SC + ch * 8);
        SCHED();
#pragma unroll
        for (int ks = 0; ks < 4; ++ks) d0 = __builtin_amdgcn_mfma_f32_16x16x32_f16(fa[ks], fb[ks], d0, 0, 0, 0);
#pragma unroll
        for (int ks = 4; ks < 8; ++ks) d1 = __builtin_amdgcn_mfma_f32_16x16x32_f16(fa[ks], fb[ks], d1, 0, 0, 0);
        LAS char* tp = lds + L_T + ch * 3456 + (16 * rtile + 4 * g) * 144 + (16 * ctile + fr) * 4;
        if (rtile * 2 + (g >> 1) < 3) {
#pragma unroll
            for (int i = 0; i < 4; ++i) *(LAS float*)(tp + i * 144) = d0[i] * sc.x + d1[i] * sc.y;
        }
    }
    BAR();
    STAMPA(5);
    {
        asm volatile("s_waitcnt vmcnt(%2)" : "+v"(tb0), "+v"(tb1) : "i"(4) : "memory");
        const int ch = tid >> 8, mu = tid & 255;
        LAS char* tp = lds + L_T + ch * 3456;
        const u32 o[11] = {tb0.x & 0xffffu, tb0.x >> 16, tb0.y & 0xffffu, tb0.y >> 16, tb0.z & 0xffffu, tb0.z >> 16,
                           tb0.w & 0xffffu, tb0.w >> 16, tb1.x & 0xffffu, tb1.x >> 16, tb1.y & 0xffffu};
        float V[11];
#pragma unroll
        for (int k = 0; k < 11; ++k) V[k] = *(LAS float*)(tp + o[k]);
        if (mu == 0) { V[0] = 0.f; V[1] = 0.f; V[2] = 0.f; }
        u32 P[11];
#pragma unroll
        for (int k = 1; k < 11; ++k) P[k] = pkbf(V[k], V[k - 1]);
        LAS char* kp = lds + L_KC + ch * 16640 + (256 - mu) * 16;
#pragma unroll
        for (int a = 0; a < 4; ++a) { u32x4 c = {P[7 + a], P[5 + a], P[3 + a], P[1 + a]}; *(LAS u32x4*)(kp + a * 4160) = c; }
        const u32x4 z = {0, 0, 0, 0};
        if (tid < 154) *(LAS u32x4*)(lds + zaddr) = z;
    }
    STAMPA(6);
    float xs[2][4];
    {
        u32x4 a0, a1, s0, s1;
        const u32 ox = WS_XT + (u32)(b * 16384 + wave * 2048 + lane * 16);
        const u32 os = WS_STATS + (u32)((wave * 32 + (lane >> 2)) * 128 + (lane & 3) * 16);
#pragma nounroll
        for (int spin = 0; spin < SPIN_CAP; ++spin) {
            asm volatile("global_load_dwordx4 %0, %4, %6 sc1\n\tglobal_load_dwordx4 %1, %4, %6 offset:1024 sc1\n\t"
                         "global_load_dwordx4 %2, %5, %6 sc1\n\tglobal_load_dwordx4 %3, %5, %6 offset:2048 sc1\n\ts_waitcnt vmcnt(0)"
                         : "=&v"(a0), "=&v"(a1), "=&v"(s0), "=&v"(s1) : "v"(ox), "v"(os), "s"(ws) : "memory");
            const bool ok = a0.y == tag && a0.w == tag && a1.y == tag && a1.w == tag && s0.y == tag && s0.w == tag && s1.y == tag && s1.w == tag;
            if (__all(ok)) break;
            __builtin_amdgcn_s_sleep(8);
        }
        asm volatile("" :: "v"(warm));
        STAMPA(7);
        const u32 xv[4] = {a0.x, a0.z, a1.x, a1.z}, sv[4] = {s0.x, s0.z, s1.x, s1.z};
        const float D0 = bflo(dskp), D1 = bfhi(dskp);
        float u0[4], u1[4];
#pragma unroll
        for (int k = 0; k < 4; ++k) {
            const float m = bflo(sv[k]), ri = __builtin_amdgcn_rcpf(bfhi(sv[k]));
            xs[0][k] = bflo(xv[k]); xs[1][k] = bfhi(xv[k]);
            const u32 dm = pkbf(xs[0][k] - m, xs[1][k] - m);
            u0[k] = bflo(dm) * ri; u1[k] = bfhi(dm) * ri;
        }
        LAS char* up = lds + L_UC + (wave * 256 + 2 * lane) * 2;
#pragma unroll
        for (int gq = 0; gq < 2; ++gq) {
            const u32 c0 = pkbf(u0[2 * gq], u0[2 * gq + 1]), c1 = pkbf(u1[2 * gq], u1[2 * gq + 1]);
            *(LAS u32*)(up + gq * 256 + 520) = c0;  *(LAS u32*)(up + gq * 256 + 4640 + 512) = c0;
            *(LAS u32*)(up + gq * 256 + 9280 + 520) = c1;  *(LAS u32*)(up + gq * 256 + 9280 + 4640 + 512) = c1;
            xs[0][2 * gq] = __builtin_fmaf(bflo(c0), D0, xs[0][2 * gq]); xs[0][2 * gq + 1] = __builtin_fmaf(bfhi(c0), D0, xs[0][2 * gq + 1]);
            xs[1][2 * gq] = __builtin_fmaf(bflo(c1), D1, xs[1][2 * gq]); xs[1][2 * gq + 1] = __builtin_fmaf(bfhi(c1), D1, xs[1][2 * gq + 1]);
        }
    }
    const int sr = (xj >> 1) * 8 + (xj & 1) * 4 + (xi - 28);
    u32x4 wv[8];
    if (!g1on) {
        const char* csrc = (const char*)w1 + (sr < 28 ? sr : 0) * 128 + (size_t)wave * 64 * 3584;
        const u32 cvo = (u32)((lane >> 3) * 3584 + (lane & 7) * 16);
#pragma unroll
        for (int k = 0; k < 8; ++k) asm volatile("global_load_dwordx4 %0, %1, %2" : "=v"(wv[k]) : "v"(cvo), "s"(csrc + k * 8 * 3584) : "memory");
    }
    BAR();
    STAMP(8);
    if (T2L == 99) {
        asm volatile("global_load_dword %0, %1, %2" : "=v"(warm) : "v"((u32)(xi * 57344 + (wave * 56 + (lane < 56 ? lane : 55)) * 128)), "s"(w2) : "memory");
    } else if (T2L == 98) {
        const int l = wave * 22 + (lane < 22 ? lane : 21), row = xi * 22 + (l >> 3);
        asm volatile("global_load_dword %0, %1, %2" : "=v"(warm) : "v"((u32)(((row / 176) * 448 + 272 + row % 176) * 1024 + (l & 7) * 128)), "s"(w2) : "memory");
    } else if (T2L) {
        const int wl = lane < T2L ? lane : T2L - 1;
        asm volatile("global_load_dword %0, %1, %2" : "=v"(warm)
                     : "v"((u32)(((wave & 3) * 448 + 448 - 8 * T2L + (wave >> 2) * 4 * T2L + (xi & 3) * T2L + wl) * 1024 + cd * 128)), "s"(w2) : "memory");
    }
    {
        const int ch = wave >> 2, rh = (wave >> 1) & 1, set = wave & 1, fr = lane & 15, g = lane >> 4;
        LAS char* pa = lds + L_KC + ch * 16640 + (fr & 3) * 4160 + 2 * (2048 - 8 * (fr >> 2) + 8 * g) - 3584 - 192 - rh * 256;
        LAS char* pb = lds + L_UC + ch * 9280 + (fr & 1) * 4640 + 2 * (256 + 32 * (fr >> 1) + 8 * g) - 192 - rh * 256;
        f32x4 acc[4] = {};
        const char* wsrc = w1c + (size_t)(wave * 48) * 3584;
        const u32 wdst = lds0 + 65536 + wave * 12288;
        if (set) conv_body<1>(pa, pb, acc, g1on, wsrc, w1v, wdst); else conv_body<0>(pa, pb, acc, g1on, wsrc, w1v, wdst);
        STAMP(9);
        BAR();
        const int blk = fr >> 1;
        LAS char* yp = lds + L_Y + (rh * 2 + ch) * 8192 + blk * 128 + (((2 * g + (fr & 1)) ^ (2 * (blk & 3))) * 16);
        *(LAS f32x4*)(yp + (set ? 6 : 7) * 1024) = acc[0];
        *(LAS f32x4*)(yp + (set ? 5 : 4) * 1024) = acc[1];
        *(LAS f32x4*)(yp + (set ? 2 : 3) * 1024) = acc[2];
        *(LAS f32x4*)(yp + (set ? 1 : 0) * 1024) = acc[3];
    }
    BAR();
    {
        const int blk = lane >> 4;
        LAS char* yp = lds + L_Y + wave * 1024 + blk * 128 + ((((lane >> 1) & 7) ^ (2 * blk)) * 16) + (lane & 1) * 8;
        f32x2 p[4], q[4];
#pragma unroll
        for (int k = 0; k < 4; ++k) { p[k] = *(LAS f32x2*)(yp + (k >> 1) * 512 + (k & 1) * 8192); q[k] = *(LAS f32x2*)(yp + (k >> 1) * 512 + (k & 1) * 8192 + 16384); }
        u32 hb[4];
#pragma unroll
        for (int k = 0; k < 4; ++k) hb[k] = pkbf(xs[k & 1][2 * (k >> 1)] + (p[k].x + q[k].x), xs[k & 1][2 * (k >> 1) + 1] + (p[k].y + q[k].y));
        const u32 o = WS_H1 + (u32)(wave * 262144 + ((lane >> 4) & 1) * 65536 + b * 256 + (lane >> 5) * 64 + (lane & 15) * 4);
        asm volatile("global_store_dword %0, %2, %6 sc1\n\tglobal_store_dword %0, %3, %6 offset:128 sc1\n\t"
                     "global_store_dword %1, %4, %6 sc1\n\tglobal_store_dword %1, %5, %6 offset:128 sc1"
                     :: "v"(o), "v"(o + 131072u), "v"(hb[0]), "v"(hb[1]), "v"(hb[2]), "v"(hb[3]), "s"(ws) : "memory");
    }
    STAMP(10);
    if (g1on) {
#pragma unroll
        for (int i = 0; i < 4; ++i) dma16(w1c + (size_t)(384 + wave * 16 + i * 4) * 3584, w1v, lds0 + 32768 + (wave * 4 + i) * 1024);
        VMW(4);
    } else VMW(0);
    asm volatile("" :: "v"(warm));
    BAR();
    if (tid == 0) asm volatile("global_store_dword %0, %1, %2 sc1" :: "v"(WS_FLAG1 + (u32)b * 128u), "v"(tag), "s"(ws) : "memory");
    STAMP(11);
    const int wq = wave & 3, wh = wave >> 2;
    const int tq = (lane & 15) >> 2, tp_ = lane & 3, tG = (lane >> 4) & 1, th = lane >> 5;
    const u32 sw128 = (u32)((((lane & 7) ^ (((lane >> 4) & 1) << 2)) * 16));
    const u32 w2v = (u32)((lane >> 3) * 1024) + sw128;
    const char* w2c = (const char*)w2 + cd * 128 + (size_t)(wq * 448) * 1024;
#define W2SLOT(s) ((s) < 3 ? 131072u + (s) * 8192u : (s) < 19 ? ((s) - 3) * 8192u : (s) < 22 ? 131072u + ((s) - 19) * 8192u : ((s) - 22) * 8192u)
#define W2DMA(s, half) dma16(w2c + (s) * 16384 + (half) * 8192, w2v, lds0 + W2SLOT(s) + wq * 2048 + (half) * 1024)
#define WAIT_FLAG1() do { if (wave < 4) { \
        const u32 of = WS_FLAG1 + (u32)tid * 128u; \
        _Pragma("nounroll") for (int spin = 0; spin < SPIN_CAP; ++spin) { \
            u32 f0; \
            asm volatile("global_load_dword %0, %1, %2 sc1\n\ts_waitcnt vmcnt(0)" : "=&v"(f0) : "v"(of), "s"(ws) : "memory"); \
            if (__all(f0 == tag)) break; \
            __builtin_amdgcn_s_sleep(2); \
        } } BAR(); } while (0)
    constexpr int PF = PFD;
    const u32 vo16 = (u32)lane * 16u;
    const char* h2src = ws + WS_HH + (size_t)((r64 * 4 + wq) * 2 + wh) * 28672;
    u32x4 hv[28];
    u32x2 gd;
#define HLOAD(i) asm volatile("global_load_dwordx4 %0, %1, %2 offset:%3 sc1" : "=v"(hv[i]) : "v"(vo16), "s"(h2src + ((i) >> 2) * 4096), "i"(((i) & 3) * 1024) : "memory")
    const u32 la = (u32)(wq * 2048 + (4 * th + tq) * 128 + (((2 * tG + (tp_ >> 1)) ^ ((tq >> 1) << 2)) * 16) + 8 * (tp_ & 1));
    bf16x8 AF[28][2];
#define AFRAG(n) do { const u32 a_ = la + W2SLOT(n); AF[n][0] = trfrag(lds + a_, lds + a_ + 1024); AF[n][1] = trfrag(lds + (a_ ^ 64u), lds + (a_ ^ 64u) + 1024); } while (0)
    if (g1on) {
        bf16x8 WA[32];
        {
            const u32 wa = (u32)((8 * th + tq) * 256 + (((4 * wq + 2 * tG + (tp_ >> 1)) ^ (tq << 2)) * 16) + 8 * (tp_ & 1)) + 65536u;
#pragma unroll
            for (int i = 0; i < 24; ++i) { LAS char* a = lds + (wa + (i >= 16 ? 65536u : 0u)) + (i & 15) * 4096; WA[i] = trfrag(a, a + 1024); }
            VMW(0);
            BAR();
#pragma unroll
            for (int i = 24; i < 32; ++i) { LAS char* a = lds + (wa - 32768u) + (i - 24) * 4096; WA[i] = trfrag(a, a + 1024); }
        }
        STAMP(12);
        WAIT_FLAG1();
        STAMP(13);
        const char* hsrc = ws + WS_H1 + (size_t)rt1 * 131072 + wave * 2048;
        const u32 hv_ = (u32)((lane >> 3) * 128) + sw128;
#define HDMA(k) dma16_dev(hsrc + ((k) >> 1) * 16384 + ((k) & 1) * 1024, hv_, lds0 + ((k) >> 1) * 16384 + wave * 2048 + ((k) & 1) * 1024)
#pragma unroll
        for (int k = 0; k < 6; ++k) HDMA(k);
        f32x16 acc0 = {}, acc1 = {};
        u32 lb = (u32)((8 * th + tq) * 128 + (((4 * wh + 2 * tG + (tp_ >> 1)) ^ ((tq >> 1) << 2)) * 16) + 8 * (tp_ & 1));
        constexpr int BD = BDD;
        bf16x8 Bf[64];
        u32x2 g0, g1; u32x4 gc;
        float cf[16];
        const u32 oc = WS_CS + (u32)((ct * 128 + wq * 32) * 8 + (lane & 15) * 16);
        const u32 og = WS_ST2 + (u32)((rt1 * 128 + wh * 64 + (lane & 31)) * 8);
        const int ks = ct * 8 + wq * 2, kqp = ks / 28;
        const char* hdst = ws + WS_HH + (size_t)(((rt1 * 2 + wh) * 4 + kqp) * 56 + (ks - kqp * 28)) * 1024;
#define G1_STORE(acc, gm, off) do { u32x4 hq[2]; const float mu_ = -bflo((gm).x); \
            _Pragma("unroll") for (int k = 0; k < 8; ++k) { \
                const u32 pk = pkbf(__builtin_fmaf(mu_, cf[2 * k], acc[2 * k]), __builtin_fmaf(mu_, cf[2 * k + 1], acc[2 * k + 1])); \
                const s16x2 zz = {0, 0}; \
                hq[k >> 2][k & 3] = __builtin_bit_cast(u32, __builtin_elementwise_max(__builtin_bit_cast(s16x2, pk), zz)); } \
            asm volatile("global_store_dwordx4 %0, %1, %3 sc1\n\tglobal_store_dwordx4 %0, %2, %3 offset:1024 sc1\n\ts_nop 1" \
                         :: "v"(vo16), "v"(hq[0]), "v"(hq[1]), "s"(hdst + (off)) : "memory"); } while (0)
        VMW(4);
        BAR();
        STAMP(14);
#pragma unroll
        for (int r = 0; r < BD; ++r) Bf[r] = trfrag(lds + lb + r * 2048, lds + lb + r * 2048 + 512);
#pragma unroll
        for (int c = 0; c < 8; ++c) {
            if (c < 6) VMW(2 + 2 * c); else if (c == 6) VMW(16);
            asm volatile("s_barrier" ::: "memory");

#pragma unroll
            for (int r = 0; r < 8; ++r) {
                const int gs = 8 * c + r, gn = gs + BD;
                if (gn == 32) lb += 65536u;
                if (gn < 64) Bf[gn < 64 ? gn : 63] = trfrag(lds + lb + (gn & 31) * 2048, lds + lb + (gn & 31) * 2048 + 512);
                if (c < 4) acc0 = __builtin_amdgcn_mfma_f32_32x32x16_bf16(WA[gs & 31], Bf[gs], acc0, 0, 0, 0);
                else acc1 = __builtin_amdgcn_mfma_f32_32x32x16_bf16(WA[gs & 31], Bf[gs], acc1, 0, 0, 0);
                if (AFIN && c == 7) AFRAG(r);
                if (AFIN && c == 7 && r == 7) AFRAG(8);
                if ((r & 1) == 0) {
                    const int n = 4 * c + (r >> 1);
                    if (n == 23)
                        asm volatile("global_load_dwordx4 %2, %4, %5 sc1\n\tglobal_load_dwordx2 %0, %3, %5 sc1\n\tglobal_load_dwordx2 %1, %3, %5 offset:256 sc1"
                                     : "=&v"(g0), "=&v"(g1), "=&v"(gc) : "v"(og), "v"(oc), "s"(ws) : "memory");
                    if (n < 10) HDMA(6 + n);
                    else if (n < 23) W2DMA(n - 10, wh);
                    else if (n == 24 || n == 25) W2DMA(n - 11, wh);
                    else if (n == 28 || n == 29) W2DMA(n - 13, wh);
                }
                SCHED();
            }
            if (c == 6) {
                STAMPB(3);
                asm volatile("s_waitcnt vmcnt(2)" : "+v"(g0), "+v"(g1), "+v"(gc) :: "memory");
#pragma nounroll
                for (int spin = 0; spin < SPIN_CAP; ++spin) {
                    if (__all(g0.y == tag && g1.y == tag && gc.y == tag && gc.w == tag)) break;
                    __builtin_amdgcn_s_sleep(2);
                    asm volatile("global_load_dwordx2 %0, %3, %5 sc1\n\tglobal_load_dwordx2 %1, %3, %5 offset:256 sc1\n\tglobal_load_dwordx4 %2, %4, %5 sc1\n\ts_waitcnt vmcnt(0)"
                                 : "=&v"(g0), "=&v"(g1), "=&v"(gc) : "v"(og), "v"(oc), "s"(ws) : "memory");
                }
                STAMPB(4);
                const u32x2 c2 = {gc.x, gc.z};
                *(LAS u32x2*)(lds + L_SCR + wave * 128 + (lane & 15) * 8) = c2;
#pragma unroll
                for (int k = 0; k < 4; ++k) {
                    const f32x4 c0 = *(LAS f32x4*)(lds + L_SCR + wave * 128 + (8 * k + 4 * th) * 4);
#pragma unroll
                    for (int e = 0; e < 4; ++e) cf[4 * k + e] = c0[e];
                }
                G1_STORE(acc0, g0, 0);
                SCHED();
            }
        }
        STAMP(15);
        G1_STORE(acc1, g1, 28672);
        STAMPB(5);
        if (!AFIN) {
#pragma unroll
            for (int i = 0; i < 9; ++i) AFRAG(i);
        }
        VMW(0);
    } else {
        STAMPB(3); STAMPB(4); STAMPB(5);
        if (sr < 28) {
            float cs[8] = {};
#pragma unroll
            for (int k = 0; k < 8; ++k) {
                asm volatile("" : "+v"(wv[k]));
#pragma unroll
                for (int e = 0; e < 4; ++e) { const u32 d = wv[k][e]; cs[2 * e] += bflo(d); cs[2 * e + 1] += bfhi(d); }
            }
#pragma unroll
            for (int e = 0; e < 8; ++e) cs[e] = dpp_add<0x128>(cs[e]);
            if ((lane & 8) == 0) {
                LAS char* pp = lds + (wave * 4 + (lane >> 4)) * 256 + (lane & 7) * 32;
                const f32x4 v0 = {cs[0], cs[1], cs[2], cs[3]}, v1 = {cs[4], cs[5], cs[6], cs[7]};
                *(LAS f32x4*)(pp) = v0; *(LAS f32x4*)(pp + 16) = v1;
            }
            BAR();
            if (wave == 7) {
                float S = 0.f;
#pragma unroll
                for (int k = 0; k < 32; ++k) S += *(LAS float*)(lds + k * 256 + lane * 4);
                const u32x2 g = {__float_as_uint(S), tag};
                asm volatile("global_store_dwordx2 %0, %1, %2 sc1" :: "v"(WS_CS + (u32)((sr * 64 + lane) * 8)), "v"(g), "s"(ws) : "memory");
            }
        }
        STAMP(12);
        WAIT_FLAG1();
        STAMP(13);
        const char* src = ws + WS_H1 + (size_t)sr * 65536 + wave * 8192;
        u32x4 hs[8];
#pragma unroll
        for (int k = 0; k < 8; ++k) asm volatile("global_load_dwordx4 %0, %1, %2 offset:%3 sc1" : "=v"(hs[k]) : "v"(vo16), "s"(src + (k >> 2) * 4096), "i"((k & 3) * 1024) : "memory");
        STAMP(14);
        float sm[8] = {}, sq[8] = {};
#pragma unroll
        for (int k = 0; k < 8; ++k) {
            asm volatile("s_waitcnt vmcnt(%1)" : "+v"(hs[k]) : "i"(7 - k) : "memory");
#pragma unroll
            for (int e = 0; e < 4; ++e) {
                const u32 d = hs[k][e];
                const float lo = bflo(d), hi = bfhi(d);
                sm[2 * e] += lo; sq[2 * e] = __builtin_fmaf(lo, lo, sq[2 * e]); sm[2 * e + 1] += hi; sq[2 * e + 1] = __builtin_fmaf(hi, hi, sq[2 * e + 1]);
            }
        }
#pragma unroll
        for (int e = 0; e < 8; ++e) { sm[e] = dpp_add<0x128>(sm[e]); sq[e] = dpp_add<0x128>(sq[e]); }
        if ((lane & 8) == 0) {
            LAS char* pp = lds + 114688 + (wave * 4 + (lane >> 4)) * 512 + (lane & 7) * 64;
#pragma unroll
            for (int e = 0; e < 4; ++e) { const f32x4 v = {sm[2 * e], sq[2 * e], sm[2 * e + 1], sq[2 * e + 1]}; *(LAS f32x4*)(pp + e * 16) = v; }
        }
        BAR();
        if (wave == 0) {
            float S = 0.f, Q = 0.f;
#pragma unroll
            for (int k = 0; k < 32; ++k) { const f32x2 v = *(LAS f32x2*)(lds + 114688 + k * 512 + lane * 8); S += v.x; Q += v.y; }
            const float mean = S * (1.f / 512.f), var = fmaxf(Q * (1.f / 512.f) - mean * mean, 0.f);
            const u32x2 g = {pkbf(mean, __builtin_amdgcn_sqrtf(rbf(rbf(var) + 1e-5f))), tag};
            asm volatile("global_store_dwordx2 %0, %1, %2 sc1" :: "v"(WS_ST2 + (u32)(((sr >> 1) * 128 + (lane >> 5) * 64 + (sr & 1) * 32 + (lane & 31)) * 8)), "v"(g), "s"(ws) : "memory");
        }
        STAMP(15);
#pragma unroll
        for (int s = 0; s < 17; ++s) { W2DMA(s, wh); __builtin_amdgcn_s_sleep(SLP2); }
        VMW(0);
        BAR();
#pragma unroll
        for (int i = 0; i < 9; ++i) AFRAG(i);
    }
    BAR();
    STAMPB(6);
    if (b == 0 && tid == 64) asm volatile("global_store_dword %0, %1, %2 sc1" :: "v"(0u), "v"(tag), "s"(ws) : "memory");
    f32x16 o0 = {}, o1 = {};
    u32x4 rs;
    if (g1on && tid == 0) asm volatile("global_store_dword %0, %1, %2 sc1" :: "v"(WS_FLAG2 + (u32)(rt1 * 14 + ct) * 128u), "v"(tag), "s"(ws) : "memory");
    STAMP(16);
#pragma unroll
    for (int s = 17; s < 28; ++s) W2DMA(s, wh);
    dma16_dev(ws + WS_H1 + (size_t)(r64 >> 1) * 131072 + (size_t)(cd * 64 + wq * 16) * 128 + wh * 65536, (u32)((lane >> 2) * 128 + (r64 & 1) * 64 + (lane & 3) * 16), lds0 + L_SCR + wave * 1024);
    STAMPB(1);
    if (wave == 7) {
        const u32 ofl = WS_FLAG2 + (u32)((r64 >> 1) * 14 + (lane < 14 ? lane : 0)) * 128u;
#pragma nounroll
        for (int spin = 0; spin < SPIN_CAP; ++spin) {
            u32 fl;
            asm volatile("global_load_dword %0, %1, %2 sc1\n\ts_waitcnt vmcnt(0)" : "=&v"(fl) : "v"(ofl), "s"(ws) : "memory");
            if (__all(fl == tag)) break;
            __builtin_amdgcn_s_sleep(4);
        }
    }
    VMW(0);
    STAMPB(7);
    asm volatile("s_barrier" ::: "memory");
    STAMPB(2);
    asm volatile("global_load_dwordx2 %0, %1, %2 sc1" : "=v"(gd) : "v"(WS_ST2 + (u32)((r64 * 64 + wh * 32 + (lane & 31)) * 8)), "s"(ws) : "memory");
#pragma unroll
    for (int i = 0; i < PF; ++i) HLOAD(i);
    STAMP(17);
#pragma unroll
    for (int i = 0; i < 28; ++i) {
        if (LKS && i % LKS == 0) asm volatile("s_barrier" ::: "memory");
        if (i + 3 >= 9 && i + 3 < 28) AFRAG(i + 3 < 28 ? i + 3 : 27);
        asm volatile("s_waitcnt vmcnt(%1)" : "+v"(hv[i]) : "i"(i + PF <= 28 ? PF - 1 : 27 - i) : "memory");
        if (i + PF < 28) HLOAD(i + PF < 28 ? i + PF : 27);
        const bf16x8 Bq = __builtin_bit_cast(bf16x8, hv[i]);
        o0 = __builtin_amdgcn_mfma_f32_32x32x16_bf16(AF[i][0], Bq, o0, 0, 0, 0);
        o1 = __builtin_amdgcn_mfma_f32_32x32x16_bf16(AF[i][1], Bq, o1, 0, 0, 0);
        SCHED();
    }
    STAMP(18);
    {
        const u32 a = L_SCR + (u32)(wh * 4096 + (wq * 16 + 4 * th + tq) * 64 + tG * 32 + tp_ * 8);
        rs = __builtin_bit_cast(u32x4, trfrag(lds + a, lds + a + 512));
    }
    asm volatile("" : "+v"(gd));
    const float ri = __builtin_amdgcn_rcpf(bfhi(gd.x));
    u16* op = out + (size_t)(r64 * 64 + wh * 32 + (lane & 31)) * 512 + cd * 64 + wq * 16 + th * 8;
    BAR();
    {
        LAS char* xp = lds + wh * 24576 + lane * 16 - 2048;
        f32x4 s0, s1;
#pragma unroll
        for (int dq = 0; dq < 4; ++dq) {
            const f32x16& o = (dq >> 1) ? o1 : o0;
            const int r = 8 * (dq & 1);
            const f32x4 v0 = {o[r], o[r + 1], o[r + 2], o[r + 3]}, v1 = {o[r + 4], o[r + 5], o[r + 6], o[r + 7]};
            if (dq == wq) { s0 = v0; s1 = v1; }
            else { LAS char* wp = xp + dq * 6144 + (wq ^ dq) * 2048; *(LAS f32x4*)(wp) = v0; *(LAS f32x4*)(wp + 1024) = v1; }
        }
        BAR();
        LAS char* rp = xp + wq * 6144;
#pragma unroll
        for (int s = 1; s < 4; ++s) { s0 += *(LAS f32x4*)(rp + s * 2048); s1 += *(LAS f32x4*)(rp + s * 2048 + 1024); }
        u32 p0 = pkbf(__builtin_fmaf(s0[0], ri, bflo(rs.x)), __builtin_fmaf(s0[1], ri, bfhi(rs.x))), p1 = pkbf(__builtin_fmaf(s0[2], ri, bflo(rs.y)), __builtin_fmaf(s0[3], ri, bfhi(rs.y)));
        u32 p2 = pkbf(__builtin_fmaf(s1[0], ri, bflo(rs.z)), __builtin_fmaf(s1[1], ri, bfhi(rs.z))), p3 = pkbf(__builtin_fmaf(s1[2], ri, bflo(rs.w)), __builtin_fmaf(s1[3], ri, bfhi(rs.w)));
        auto y0 = __builtin_amdgcn_permlane32_swap(p0, p2, false, false);
        auto y1 = __builtin_amdgcn_permlane32_swap(p1, p3, false, false);
        const u32x4 ov = {y0[0], y1[0], y0[1], y1[1]};
        *(u32x4*)op = ov;
    }
    STAMP(19);
}

extern "C" void kernel_launch(void* const* d_in, const int* in_sizes, int n_in, void* d_out, int out_size, void* d_ws, size_t ws_size,
                              hipStream_t stream) {
    (void)hipFuncSetAttribute((const void*)fused_s4d_mlp, hipFuncAttributeMaxDynamicSharedMemorySize, LDS_BYTES);
    fused_s4d_mlp<<<256, 512, LDS_BYTES, stream>>>((const u16*)d_in[0], (const u16*)d_in[1], (const u16*)d_in[2], (const u16*)d_in[3],
                                                    (const u16*)d_in[4], (const u16*)d_in[5], (const u16*)d_in[6], (const u16*)d_in[8],
                                                    (const u16*)d_in[9], (u16*)d_out, (char*)d_ws);
}
```

```cpp
#include <hip/hip_runtime.h>
#include <stdint.h>

typedef unsigned short u16;
typedef unsigned u32;
typedef u32 u32x4 __attribute__((ext_vector_type(4)));
typedef u32 u32x2 __attribute__((ext_vector_type(2)));
typedef float f32x2 __attribute__((ext_vector_type(2)));
typedef float f32x4 __attribute__((ext_vector_type(4)));
typedef float f32x16 __attribute__((ext_vector_type(16)));
typedef __bf16 bf16x2 __attribute__((ext_vector_type(2)));
typedef __bf16 bf16x8 __attribute__((ext_vector_type(8)));
typedef _Float16 f16x2 __attribute__((ext_vector_type(2)));
typedef _Float16 f16x8 __attribute__((ext_vector_type(8)));
typedef short s16x2 __attribute__((ext_vector_type(2)));
typedef short s16x4 __attribute__((ext_vector_type(4)));
#define LAS __attribute__((address_space(3)))

constexpr u32 WS_STATS = 4096;
constexpr u32 WS_FLAG1 = 40960;
constexpr u32 WS_FLAG2 = 77824;
constexpr u32 WS_ST2   = 110592;
constexpr u32 WS_CS    = 131072;
constexpr u32 WS_H1    = 147456;
constexpr u32 WS_XT    = WS_H1 + (2u << 20);
constexpr u32 WS_HH    = WS_XT + (4u << 20);
constexpr u32 WS_STAMP = 200u << 20;

constexpr u32 TROW = 544, TCH = 53 * 544;
constexpr u32 L_KC    = 0;
constexpr u32 L_UC    = 33280;
constexpr u32 L_T     = 57664;
constexpr u32 L_SC    = 64576;
constexpr u32 L_XTILE = 65536;
constexpr u32 L_Y     = 0;
constexpr u32 L_RES   = 155648;
constexpr u32 LDS_BYTES = 163840;
constexpr u32 CODE_LINES = 182;

struct ExpTab { u16 v[256 * 16]; };
constexpr ExpTab make_exptab() {
    ExpTab t{};
    for (int mu = 0; mu < 256; ++mu)
        for (int k = 0; k < 16; ++k) {
            int l = 8 * mu + k - 3, off = 0;
            if (k < 11 && l >= 0) {
                int s = (l >= 1024) + (l >= 512) + (l >= 256);
                int pos = s ? (((l + (1 << (s - 1)) - 1 + ((l >> s) & 1)) >> s) << s) : l;
                int i = pos >> 8, col = (pos >> 3) & 31, bb = pos & 7;
                int row = i == 0 ? bb : i == 1 ? 8 + bb / 2 : i < 4 ? 12 + 2 * (i - 2) + bb / 4 : 12 + i;
                off = row * 144 + col * 4;
            }
            t.v[mu * 16 + k] = (u16)off;
        }
    return t;
}
static __device__ const ExpTab g_exptab = make_exptab();

#define BAR() asm volatile("s_waitcnt lgkmcnt(0)\n\ts_barrier" ::: "memory")
#define SBAR() asm volatile("s_barrier" ::: "memory")
#define VMW(n) asm volatile("s_waitcnt vmcnt(%0)" :: "i"(n) : "memory")
#define SCHED() __builtin_amdgcn_sched_barrier(0)
#define STAMP(k)
constexpr int SPIN_CAP = 1 << 15;

__device__ __forceinline__ float bflo(u32 v) { return __uint_as_float(v << 16); }
__device__ __forceinline__ float bfhi(u32 v) { return __uint_as_float(v & 0xffff0000u); }
__device__ __forceinline__ u32 pkbf(float a, float b) { f32x2 x = {a, b}; bf16x2 r = __builtin_convertvector(x, bf16x2); return __builtin_bit_cast(u32, r); }
__device__ __forceinline__ float rbf(float a) { return bflo(pkbf(a, a)); }
template <int CTRL> __device__ __forceinline__ float dpp_add(float v) {
    return v + __int_as_float(__builtin_amdgcn_update_dpp(0, __float_as_int(v), CTRL, 0xf, 0xf, true));
}
template <int CTRL> __device__ __forceinline__ u32 dpp_umax(u32 v) { return max(v, (u32)__builtin_amdgcn_update_dpp(0, (int)v, CTRL, 0xf, 0xf, true)); }
__device__ __forceinline__ u32 wave_umax(u32 v) {
    v = dpp_umax<0xb1>(v); v = dpp_umax<0x4e>(v); v = dpp_umax<0x141>(v); v = dpp_umax<0x140>(v);
    return max(max((u32)__builtin_amdgcn_readlane((int)v, 0), (u32)__builtin_amdgcn_readlane((int)v, 16)),
               max((u32)__builtin_amdgcn_readlane((int)v, 32), (u32)__builtin_amdgcn_readlane((int)v, 48)));
}
__device__ __forceinline__ void dma16(const void* sbase, u32 voff, u32 lds) {
    asm volatile("s_mov_b32 m0, %2\n\ts_nop 0\n\tglobal_load_lds_dwordx4 %0, %1" :: "v"(voff), "s"(sbase), "s"(lds) : "memory");
}
__device__ __forceinline__ void dma16_dev(const void* sbase, u32 voff, u32 lds) {
    asm volatile("s_mov_b32 m0, %2\n\ts_nop 0\n\tglobal_load_lds_dwordx4 %0, %1 sc1" :: "v"(voff), "s"(sbase), "s"(lds) : "memory");
}
__device__ __forceinline__ void put_word(char* ws, u32 off, u32 v) { asm volatile("global_store_dword %0, %1, %2 sc1" :: "v"(off), "v"(v), "s"(ws) : "memory"); }
__device__ __forceinline__ void put_granule(char* ws, u32 off, u32x2 g) { asm volatile("global_store_dwordx2 %0, %1, %2 sc1" :: "v"(off), "v"(g), "s"(ws) : "memory"); }
template <int SLEEP> __device__ __forceinline__ void wait_flags(char* ws, u32 off, u32 tag) {
#pragma nounroll
    for (int spin = 0; spin < SPIN_CAP; ++spin) {
        u32 f;
        asm volatile("global_load_dword %0, %1, %2 sc1\n\ts_waitcnt vmcnt(0)" : "=&v"(f) : "v"(off), "s"(ws) : "memory");
        if (__all(f == tag)) break;
        __builtin_amdgcn_s_sleep(SLEEP);
    }
}
typedef f32x2 cplx;
__device__ __forceinline__ void cmul2(cplx& ra, cplx& rb, cplx a, cplx b, cplx w) {
    asm("v_pk_mul_f32 %0, %2, %4 op_sel_hi:[1,0]\n\t"
        "v_pk_mul_f32 %1, %3, %4 op_sel_hi:[1,0]\n\t"
        "v_pk_fma_f32 %0, %2, %4, %0 op_sel:[1,1,0] op_sel_hi:[0,1,1] neg_lo:[1,0,0]\n\t"
        "v_pk_fma_f32 %1, %3, %4, %1 op_sel:[1,1,0] op_sel_hi:[0,1,1] neg_lo:[1,0,0]"
        : "=&v"(ra), "=&v"(rb) : "v"(a), "v"(b), "v"(w));
}
__device__ __forceinline__ cplx cmul(cplx a, cplx w) {
    cplx r;
    asm("v_pk_mul_f32 %0, %1, %2 op_sel_hi:[1,0]\n\ts_nop 0\n\t"
        "v_pk_fma_f32 %0, %1, %2, %0 op_sel:[1,1,0] op_sel_hi:[0,1,1] neg_lo:[1,0,0]"
        : "=&v"(r) : "v"(a), "v"(w));
    return r;
}
__device__ __forceinline__ u32 pkh(cplx x) { f16x2 r = __builtin_convertvector(x, f16x2); return __builtin_bit_cast(u32, r); }
__device__ __forceinline__ float exp_c(float x) {
    const float t = x * 1.44269504088896341f;
    const float r = __builtin_fmaf(x, 1.44269504088896341f, -t) + x * 1.92596299e-8f;
    const float e = __builtin_amdgcn_exp2f(t);
    return __builtin_fmaf(e * r, 0.693147180559945f, e);
}
__device__ __forceinline__ cplx cexp_m(float zr, float zi, float m) {
    const float mag = __builtin_amdgcn_exp2f(zr * m * 1.44269504088896341f);
    const float p = zi * m;
    const float k = __builtin_rintf(p * 0.15915494309189535f);
    const float f = __builtin_fmaf(p, 0.15915494309189535f, -k) + p * 6.4206383e-9f;
    return cplx{mag * __builtin_amdgcn_cosf(f), mag * __builtin_amdgcn_sinf(f)};
}
__device__ __forceinline__ bf16x8 trfrag(LAS char* p0, LAS char* p1) {
    s16x4 a = __builtin_amdgcn_ds_read_tr16_b64_v4i16((LAS s16x4*)p0);
    s16x4 b = __builtin_amdgcn_ds_read_tr16_b64_v4i16((LAS s16x4*)p1);
    u32x2 ua = __builtin_bit_cast(u32x2, a), ub = __builtin_bit_cast(u32x2, b);
    u32x4 r = {ua.x, ua.y, ub.x, ub.y};
    return __builtin_bit_cast(bf16x8, r);
}

template <int SET> __device__ __forceinline__ void conv_body(LAS char* pa, LAS char* pb, f32x4 (&acc)[4], bool dma, const char* wsrc, u32 wv, u32 wdst) {
    constexpr int T0 = SET ? 6 : 7, T1 = SET ? 5 : 4, T2 = SET ? 2 : 3, T3 = SET ? 1 : 0;
    constexpr int NF = T0 + 1;
    bf16x8 A[2][NF], B[2][NF];
#define CV_A(a) A[0][a] = *(LAS bf16x8*)(pa + 192 + 512 * (7 - (a)))
#define CV_B(m) B[0][m] = *(LAS bf16x8*)(pb + 192 + 512 * (m))
    if (SET) { CV_A(0); CV_B(6); CV_B(5); CV_B(2); CV_B(1); CV_A(1); CV_B(4); CV_B(0); CV_A(2); CV_B(3); CV_A(3); CV_A(4); CV_A(5); CV_A(6); }
    else { CV_A(0); CV_B(NF - 1); CV_B(4); CV_B(3); CV_B(0); CV_A(1); CV_B(6); CV_B(2); CV_A(2); CV_B(5); CV_B(1); CV_A(3); CV_A(4); CV_A(5); CV_A(6); CV_A(NF - 1); }
    SCHED();
#pragma unroll
    for (int r = 0; r < 4; ++r) {
        const int cu = r & 1, nx = cu ^ 1;
#pragma unroll
        for (int a = 0; a < NF; ++a) {
            if (r < 3) { A[nx][a] = *(LAS bf16x8*)(pa + 128 - 64 * r + 512 * (7 - a)); B[nx][T0 - a] = *(LAS bf16x8*)(pb + 128 - 64 * r + 512 * (T0 - a)); }
            if (a == 1 || a == 3 || a == 5) { if (dma) dma16(wsrc + (size_t)(r * 12 + (a >> 1) * 4) * 3584, wv, wdst + (r * 3 + (a >> 1)) * 1024); }
            acc[0] = __builtin_amdgcn_mfma_f32_16x16x32_bf16(A[cu][a], B[cu][T0 - a], acc[0], 0, 0, 0);
            if (a <= T1) acc[1] = __builtin_amdgcn_mfma_f32_16x16x32_bf16(A[cu][a], B[cu][T1 - a], acc[1], 0, 0, 0);
            if (a <= T2) acc[2] = __builtin_amdgcn_mfma_f32_16x16x32_bf16(A[cu][a], B[cu][T2 - a], acc[2], 0, 0, 0);
            if (a <= T3) acc[3] = __builtin_amdgcn_mfma_f32_16x16x32_bf16(A[cu][a], B[cu][T3 - a], acc[3], 0, 0, 0);
            SCHED();
        }
    }
}

__global__ void __launch_bounds__(512, 2)
fused_s4d_mlp(const u16* __restrict__ x, const u16* __restrict__ pLogA, const u16* __restrict__ pAim, const u16* __restrict__ pCr,
              const u16* __restrict__ pCi, const u16* __restrict__ pLogdt, const u16* __restrict__ pDsk, const u16* __restrict__ w1,
              const u16* __restrict__ w2, u16* __restrict__ out, char* ws) {
    extern __shared__ __attribute__((aligned(16))) char smem_raw[];
    LAS char* lds = (LAS char*)smem_raw;
    const u32 lds0 = (u32)(uintptr_t)lds;
    const int tid = threadIdx.x, bid = blockIdx.x, lane = tid & 63;
    const int wave = __builtin_amdgcn_readfirstlane(tid >> 6);
    u32 warm0, warm1, warm2;
    {
        const char* pc;
        asm volatile("s_getpc_b64 %0" : "=s"(pc));
        asm volatile("global_load_dword %0, %1, %2" : "=v"(warm0) : "v"(min((u32)tid >> 1, CODE_LINES) * 128u), "s"(pc) : "memory");
    }
    if (wave >= 4) __builtin_amdgcn_s_setprio(1);
    STAMP(0);
    const int xj = bid & 7, xi = bid >> 3;
    const int b = xj * 32 + xi;
    const bool g1on = xi < 28;
    const int rt1 = (xj >> 1) * 4 + (xi & 3), ct = (xj & 1) * 7 + (g1on ? xi >> 2 : 0);
    const int r64 = xj * 4 + (xi & 3), cd = ((xi >> 2) & 3) == 3 ? (xi >> 2) ^ 4 : xi >> 2;
    asm volatile("" :: "s"(out), "s"(w1), "s"(w2));

    u32 Ev, ldtp, dskp, pa_, pb_, pc_, pd_;
    u32x4 xt, xr, tb0; u32x2 tb1;
    {
        const u32 o_xt = (u32)(((b >> 1) * 16 + (lane >> 2)) * 1024 + (b & 1) * 512 + wave * 64 + (lane & 3) * 16);
        const u32 o_xr = (u32)((b * 8 + wave) * 1024 + lane * 16);
        const u32 o_p = (u32)((b * 256 + (tid & 255)) * 2);
        const u32 o_tb = (u32)((tid & 255) * 32);
        asm volatile("global_load_dwordx4 %0, %1, %2" : "=v"(xt) : "v"(o_xt), "s"(x) : "memory");
        asm volatile("global_load_dword %0, %1, %2 sc1" : "=v"(Ev) : "v"(0u), "s"(ws) : "memory");
        asm volatile("global_load_dwordx4 %0, %1, %2" : "=v"(xr) : "v"(o_xr), "s"(x) : "memory");
        asm volatile("global_load_dword %0, %1, %2" : "=v"(ldtp) : "v"((u32)(b * 4)), "s"(pLogdt) : "memory");
        asm volatile("global_load_ushort %0, %1, %2" : "=v"(pa_) : "v"(o_p), "s"(pLogA) : "memory");
        asm volatile("global_load_ushort %0, %1, %2" : "=v"(pb_) : "v"(o_p), "s"(pAim) : "memory");
        asm volatile("global_load_ushort %0, %1, %2" : "=v"(pc_) : "v"(o_p), "s"(pCr) : "memory");
        asm volatile("global_load_ushort %0, %1, %2" : "=v"(pd_) : "v"(o_p), "s"(pCi) : "memory");
        asm volatile("global_load_dword %0, %1, %2" : "=v"(dskp) : "v"((u32)(b * 4)), "s"(pDsk) : "memory");
        asm volatile("global_load_dwordx4 %0, %1, %2" : "=v"(tb0) : "v"(o_tb), "s"(&g_exptab) : "memory");
        asm volatile("global_load_dwordx2 %0, %1, %2 offset:16" : "=v"(tb1) : "v"(o_tb), "s"(&g_exptab) : "memory");
    }
    const u32 w1v = (u32)((lane >> 4) * 3584 + (((lane & 15) ^ ((lane >> 4) << 2)) * 16));
    const char* w1c = (const char*)w1 + ct * 256;
    const u32 o_warm = (u32)(b * 7168 + wave * 896 + (lane < 7 ? lane : 6) * 128);
    u32 zaddr;
    {
        const int k = tid - 2;
        zaddr = tid < 2 ? L_UC + tid * 9280 + 512 : k < 128 ? L_UC + (k >> 5) * 4640 + (k & 31) * 16 : L_KC + ((k - 128) & 7) * 4160 + (257 + ((k - 128) >> 3)) * 16;
    }
    u32 a_tw = L_XTILE + (u32)(wave * 1280 + (lane >> 2) * 80 + (lane & 3) * 16), a_tr = L_XTILE + (u32)(wave * 1280 + (lane & 7) * 160 + (lane >> 3) * 4);
    u32 o_xs = WS_XT + (u32)(((b & 1) * 128 + wave * 16 + (lane >> 3)) * 16384 + ((b >> 1) * 16 + 2 * (lane & 7)) * 8), o_xs2 = o_xs + 8u * 16384u;
    u32 o_st = WS_STATS + (u32)(b * 128 + wave * 8);
    asm volatile("" : "+v"(a_tw), "+v"(a_tr), "+v"(o_xs), "+v"(o_xs2), "+v"(o_st), "+v"(zaddr));
    STAMP(1);
    asm volatile("s_waitcnt vmcnt(9)" : "+v"(xt), "+v"(Ev), "+v"(warm0) :: "memory");
    const u32 tag = (u32)__builtin_amdgcn_readfirstlane((int)Ev) + 1u;
    {
        *(LAS u32x4*)(lds + a_tw) = xt;
        LAS char* src = lds + a_tr;
        u32x4 g0 = {*(LAS u32*)(src), tag, *(LAS u32*)(src + 80), tag};
        u32x4 g1 = {*(LAS u32*)(src + 32), tag, *(LAS u32*)(src + 112), tag};
        asm volatile("global_store_dwordx4 %0, %2, %4 sc1\n\tglobal_store_dwordx4 %1, %3, %4 sc1\n\ts_nop 1" :: "v"(o_xs), "v"(o_xs2), "v"(g0), "v"(g1), "s"(ws) : "memory");
    }
    STAMP(2);
    asm volatile("s_waitcnt vmcnt(10)" : "+v"(xr) :: "memory");
    {
        const float x0 = bflo((u32)__builtin_amdgcn_readfirstlane((int)xr.x));
        const float v[8] = {bflo(xr.x) - x0, bfhi(xr.x) - x0, bflo(xr.y) - x0, bfhi(xr.y) - x0, bflo(xr.z) - x0, bfhi(xr.z) - x0, bflo(xr.w) - x0, bfhi(xr.w) - x0};
        float s1 = ((v[0] + v[1]) + (v[2] + v[3])) + ((v[4] + v[5]) + (v[6] + v[7]));
        float q1 = __builtin_fmaf(v[0], v[0], __builtin_fmaf(v[1], v[1], __builtin_fmaf(v[2], v[2], v[3] * v[3]))) +
                   __builtin_fmaf(v[4], v[4], __builtin_fmaf(v[5], v[5], __builtin_fmaf(v[6], v[6], v[7] * v[7])));
        s1 = dpp_add<0xb1>(s1); q1 = dpp_add<0xb1>(q1); s1 = dpp_add<0x4e>(s1); q1 = dpp_add<0x4e>(q1);
        s1 = dpp_add<0x141>(s1); q1 = dpp_add<0x141>(q1); s1 = dpp_add<0x140>(s1); q1 = dpp_add<0x140>(q1);
        s1 = dpp_add<0x142>(s1); q1 = dpp_add<0x142>(q1);
        auto e = __builtin_amdgcn_permlane32_swap(__float_as_uint(s1), __float_as_uint(q1), false, false);
        s1 = __uint_as_float(e[0]) + __uint_as_float(e[1]);
        const float S = __int_as_float(__builtin_amdgcn_readlane(__float_as_int(s1), 16)), Q = __int_as_float(__builtin_amdgcn_readlane(__float_as_int(s1), 48));
        const float dm = S * (1.f / 512.f);
        const float var = fmaxf(__builtin_fmaf(-dm, dm, Q * (1.f / 512.f)), 0.f);
        const float den = __builtin_amdgcn_sqrtf(rbf(rbf(var) + 1e-5f));
        if (lane == 0) put_granule(ws, o_st, u32x2{pkbf(x0 + dm, den), tag});
    }
    STAMP(3);
    asm volatile("s_waitcnt vmcnt(6)" : "+v"(pa_), "+v"(pb_), "+v"(pc_), "+v"(pd_), "+v"(ldtp) :: "memory");
    asm volatile("global_load_dword %0, %1, %2" : "=v"(warm1) : "v"(o_warm), "s"(w1) : "memory");
    {
        const int ch = (wave >> 1) & 1;
        const float dt = rbf(exp_c(ch ? bfhi(ldtp) : bflo(ldtp)));
        const float zr = -rbf(exp_c(bflo(pa_))) * dt, zi = bflo(pb_) * dt;
        LAS char* tp = lds + ch * TCH + ((wave & 1) * 64 + lane) * 4;
#define TPUT(row, v) *(LAS u32*)(tp + (row) * TROW) = pkh(v)
        if (wave < 4) {
            cplx e0 = {1.f, 0.f}, e1 = cexp_m(zr, zi, 8.f), e2 = cmul(e1, e1), e3, w4;
            cmul2(e3, w4, e1, e2, e2);
#pragma unroll
            for (int j = 0; j < 32; j += 4) {
                TPUT(21 + j, e0); TPUT(22 + j, e1); TPUT(23 + j, e2); TPUT(24 + j, e3);
                if (j < 28) { cplx n0, n1, n2, n3; cmul2(n0, n1, e0, e1, w4); cmul2(n2, n3, e2, e3, w4); e0 = n0; e1 = n1; e2 = n2; e3 = n3; }
            }
        } else {
            const u32 eb = wave_umax(max(pc_ & 0x7fffu, pd_ & 0x7fffu)) >> 7;
            const float sc = __uint_as_float((254u - eb) << 23);
            if (lane == 0) *(LAS float*)(lds + L_SC + (wave & 3) * 4) = 2.f * dt * __uint_as_float(eb << 23);
            const cplx wa = cexp_m(zr, -zi, 1.f), wq = cexp_m(zr, -zi, 256.f), wb = cmul(wa, wa);
            cplx v0 = {bflo(pc_) * sc, -bflo(pd_) * sc}, v1 = cmul(v0, wa), v2, v3, v4, v5, v6, v7, a0, a2, a4, a6;
            cmul2(v2, v3, v0, v1, wb); cmul2(v4, v5, v2, v3, wb); cmul2(v6, v7, v4, v5, wb);
            TPUT(0, v0); TPUT(1, v1); TPUT(2, v2); TPUT(3, v3); TPUT(4, v4); TPUT(5, v5); TPUT(6, v6); TPUT(7, v7);
            cmul2(a0, a4, v0, v4, wq); cmul2(a2, a6, v2, v6, wq);
            TPUT(8, a0); TPUT(9, a2); TPUT(10, a4); TPUT(11, a6);
            cmul2(v0, v4, a0, a4, wq);
            TPUT(12, v0); TPUT(13, v4);
            cmul2(a0, a4, v0, v4, wq);
            TPUT(14, a0); TPUT(15, a4);
            const cplx wq2 = cmul(wq, wq);
            v1 = cmul(a0, wq);
            cmul2(v2, v3, a0, v1, wq2);
            cmul2(v4, v5, v2, v3, wq2);
            TPUT(16, v1); TPUT(17, v2); TPUT(18, v3); TPUT(19, v4); TPUT(20, v5);
        }
    }
    STAMP(4);
    __builtin_amdgcn_s_setprio(0);
    BAR();
    {
        const int ch = wave >> 2, rtile = (wave >> 1) & 1, ctile = wave & 1, fr = lane & 15, g = lane >> 4;
        LAS char* pa = lds + ch * TCH + min(16 * rtile + fr, 20) * TROW + g * 16;
        LAS char* pb = lds + ch * TCH + (21 + 16 * ctile + fr) * TROW + g * 16;
        f32x4 d0 = {0, 0, 0, 0}, d1 = {0, 0, 0, 0};
        f16x8 fa[8], fb[8];
#pragma unroll
        for (int ks = 0; ks < 8; ++ks) { fa[ks] = *(LAS f16x8*)(pa + ks * 64); fb[ks] = *(LAS f16x8*)(pb + ks * 64); }
        const f32x2 sc = *(LAS f32x2*)(lds + L_SC + ch * 8);
        SCHED();
#pragma unroll
        for (int ks = 0; ks < 4; ++ks) d0 = __builtin_amdgcn_mfma_f32_16x16x32_f16(fa[ks], fb[ks], d0, 0, 0, 0);
#pragma unroll
        for (int ks = 4; ks < 8; ++ks) d1 = __builtin_amdgcn_mfma_f32_16x16x32_f16(fa[ks], fb[ks], d1, 0, 0, 0);
        LAS char* tp = lds + L_T + ch * 3456 + (16 * rtile + 4 * g) * 144 + (16 * ctile + fr) * 4;
        if (rtile * 2 + (g >> 1) < 3) {
#pragma unroll
            for (int i = 0; i < 4; ++i) *(LAS float*)(tp + i * 144) = d0[i] * sc.x + d1[i] * sc.y;
        }
    }
    BAR();
    STAMP(5);
    u32x4 a0, a1, s0, s1;
    const u32 ox = WS_XT + (u32)(b * 16384 + wave * 2048 + lane * 16);
    const u32 os = WS_STATS + (u32)((wave * 32 + (lane >> 2)) * 128 + (lane & 3) * 16);
    asm volatile("global_load_dwordx4 %0, %4, %6 sc1\n\tglobal_load_dwordx4 %1, %4, %6 offset:1024 sc1\n\t"
                 "global_load_dwordx4 %2, %5, %6 sc1\n\tglobal_load_dwordx4 %3, %5, %6 offset:2048 sc1"
                 : "=&v"(a0), "=&v"(a1), "=&v"(s0), "=&v"(s1) : "v"(ox), "v"(os), "s"(ws) : "memory");
    {
        asm volatile("s_waitcnt vmcnt(8)" : "+v"(tb0), "+v"(tb1), "+v"(dskp) :: "memory");
        const int ch = tid >> 8, mu = tid & 255;
        LAS char* tp = lds + L_T + ch * 3456;
        const u32 o[11] = {tb0.x & 0xffffu, tb0.x >> 16, tb0.y & 0xffffu, tb0.y >> 16, tb0.z & 0xffffu, tb0.z >> 16,
                           tb0.w & 0xffffu, tb0.w >> 16, tb1.x & 0xffffu, tb1.x >> 16, tb1.y & 0xffffu};
        float V[11];
#pragma unroll
        for (int k = 0; k < 11; ++k) V[k] = *(LAS float*)(tp + o[k]);
        if (mu == 0) { V[0] = 0.f; V[1] = 0.f; V[2] = 0.f; }
        u32 P[11];
#pragma unroll
        for (int k = 1; k < 11; ++k) P[k] = pkbf(V[k], V[k - 1]);
        LAS char* kp = lds + L_KC + ch * 16640 + (256 - mu) * 16;
#pragma unroll
        for (int a = 0; a < 4; ++a) { u32x4 c = {P[7 + a], P[5 + a], P[3 + a], P[1 + a]}; *(LAS u32x4*)(kp + a * 4160) = c; }
        const u32x4 z = {0, 0, 0, 0};
        if (tid < 154) *(LAS u32x4*)(lds + zaddr) = z;
    }
    STAMP(6);
    float xs[2][4];
    {
        asm volatile("s_waitcnt vmcnt(0)" : "+v"(a0), "+v"(a1), "+v"(s0), "+v"(s1), "+v"(warm1) :: "memory");
#pragma nounroll
        for (int spin = 0; spin < SPIN_CAP; ++spin) {
            const bool ok = a0.y == tag && a0.w == tag && a1.y == tag && a1.w == tag && s0.y == tag && s0.w == tag && s1.y == tag && s1.w == tag;
            if (__all(ok)) break;
            if (spin) __builtin_amdgcn_s_sleep(8);
            asm volatile("global_load_dwordx4 %0, %4, %6 sc1\n\tglobal_load_dwordx4 %1, %4, %6 offset:1024 sc1\n\t"
                         "global_load_dwordx4 %2, %5, %6 sc1\n\tglobal_load_dwordx4 %3, %5, %6 offset:2048 sc1\n\ts_waitcnt vmcnt(0)"
                         : "=&v"(a0), "=&v"(a1), "=&v"(s0), "=&v"(s1) : "v"(ox), "v"(os), "s"(ws) : "memory");
        }
        STAMP(7);
        const u32 xv[4] = {a0.x, a0.z, a1.x, a1.z}, sv[4] = {s0.x, s0.z, s1.x, s1.z};
        const float D0 = bflo(dskp), D1 = bfhi(dskp);
        float u0[4], u1[4];
#pragma unroll
        for (int k = 0; k < 4; ++k) {
            const float m = bflo(sv[k]), ri = __builtin_amdgcn_rcpf(bfhi(sv[k]));
            xs[0][k] = bflo(xv[k]); xs[1][k] = bfhi(xv[k]);
            const u32 dm = pkbf(xs[0][k] - m, xs[1][k] - m);
            u0[k] = bflo(dm) * ri; u1[k] = bfhi(dm) * ri;
        }
        LAS char* up = lds + L_UC + (wave * 256 + 2 * lane) * 2;
#pragma unroll
        for (int gq = 0; gq < 2; ++gq) {
            const u32 c0 = pkbf(u0[2 * gq], u0[2 * gq + 1]), c1 = pkbf(u1[2 * gq], u1[2 * gq + 1]);
            *(LAS u32*)(up + gq * 256 + 520) = c0;  *(LAS u32*)(up + gq * 256 + 4640 + 512) = c0;
            *(LAS u32*)(up + gq * 256 + 9280 + 520) = c1;  *(LAS u32*)(up + gq * 256 + 9280 + 4640 + 512) = c1;
            xs[0][2 * gq] = __builtin_fmaf(bflo(c0), D0, xs[0][2 * gq]); xs[0][2 * gq + 1] = __builtin_fmaf(bfhi(c0), D0, xs[0][2 * gq + 1]);
            xs[1][2 * gq] = __builtin_fmaf(bflo(c1), D1, xs[1][2 * gq]); xs[1][2 * gq + 1] = __builtin_fmaf(bfhi(c1), D1, xs[1][2 * gq + 1]);
        }
    }
    const int sr = (xj >> 1) * 8 + (xj & 1) * 4 + (xi - 28);
    u32x4 wv[8];
    if (!g1on) {
        const char* csrc = (const char*)w1 + (sr < 28 ? sr : 0) * 128 + (size_t)wave * 64 * 3584;
        const u32 cvo = (u32)((lane >> 3) * 3584 + (lane & 7) * 16);
#pragma unroll
        for (int k = 0; k < 8; ++k) asm volatile("global_load_dwordx4 %0, %1, %2" : "=v"(wv[k]) : "v"(cvo), "s"(csrc + k * 8 * 3584) : "memory");
    }
    BAR();
    STAMP(8);
    asm volatile("global_load_dword %0, %1, %2" : "=v"(warm2) : "v"(o_warm), "s"(w2) : "memory");
    {
        const int ch = wave >> 2, rh = (wave >> 1) & 1, set = wave & 1, fr = lane & 15, g = lane >> 4;
        LAS char* pa = lds + L_KC + ch * 16640 + (fr & 3) * 4160 + 2 * (2048 - 8 * (fr >> 2) + 8 * g) - 3584 - 192 - rh * 256;
        LAS char* pb = lds + L_UC + ch * 9280 + (fr & 1) * 4640 + 2 * (256 + 32 * (fr >> 1) + 8 * g) - 192 - rh * 256;
        f32x4 acc[4] = {};
        const char* wsrc = w1c + (size_t)(wave * 48) * 3584;
        const u32 wdst = lds0 + 65536 + wave * 12288;
        if (set) conv_body<1>(pa, pb, acc, g1on, wsrc, w1v, wdst); else conv_body<0>(pa, pb, acc, g1on, wsrc, w1v, wdst);
        STAMP(9);
        BAR();
        const int blk = fr >> 1;
        LAS char* yp = lds + L_Y + (rh * 2 + ch) * 8192 + blk * 128 + (((2 * g + (fr & 1)) ^ (2 * (blk & 3))) * 16);
        *(LAS f32x4*)(yp + (set ? 6 : 7) * 1024) = acc[0];
        *(LAS f32x4*)(yp + (set ? 5 : 4) * 1024) = acc[1];
        *(LAS f32x4*)(yp + (set ? 2 : 3) * 1024) = acc[2];
        *(LAS f32x4*)(yp + (set ? 1 : 0) * 1024) = acc[3];
    }
    BAR();
    {
        const int blk = lane >> 4;
        LAS char* yp = lds + L_Y + wave * 1024 + blk * 128 + ((((lane >> 1) & 7) ^ (2 * blk)) * 16) + (lane & 1) * 8;
        f32x2 p[4], q[4];
#pragma unroll
        for (int k = 0; k < 4; ++k) { p[k] = *(LAS f32x2*)(yp + (k >> 1) * 512 + (k & 1) * 8192); q[k] = *(LAS f32x2*)(yp + (k >> 1) * 512 + (k & 1) * 8192 + 16384); }
        u32 hb[4];
#pragma unroll
        for (int k = 0; k < 4; ++k) hb[k] = pkbf(xs[k & 1][2 * (k >> 1)] + (p[k].x + q[k].x), xs[k & 1][2 * (k >> 1) + 1] + (p[k].y + q[k].y));
        const u32 o = WS_H1 + (u32)(wave * 262144 + ((lane >> 4) & 1) * 65536 + b * 256 + (lane >> 5) * 64 + (lane & 15) * 4);
        asm volatile("global_store_dword %0, %2, %6 sc1\n\tglobal_store_dword %0, %3, %6 offset:128 sc1\n\t"
                     "global_store_dword %1, %4, %6 sc1\n\tglobal_store_dword %1, %5, %6 offset:128 sc1"
                     :: "v"(o), "v"(o + 131072u), "v"(hb[0]), "v"(hb[1]), "v"(hb[2]), "v"(hb[3]), "s"(ws) : "memory");
    }
    STAMP(10);
    if (g1on) {
#pragma unroll
        for (int i = 0; i < 4; ++i) dma16(w1c + (size_t)(384 + wave * 16 + i * 4) * 3584, w1v, lds0 + 32768 + (wave * 4 + i) * 1024);
        VMW(4);
    } else VMW(0);
    asm volatile("" :: "v"(warm2));
    BAR();
    if (tid == 0) put_word(ws, WS_FLAG1 + (u32)b * 128u, tag);
    STAMP(11);
    const int wq = wave & 3, wh = wave >> 2;
    const int tq = (lane & 15) >> 2, tp_ = lane & 3, tG = (lane >> 4) & 1, th = lane >> 5;
    const u32 sw128 = (u32)((((lane & 7) ^ (((lane >> 4) & 1) << 2)) * 16));
    const u32 w2v = (u32)((lane >> 3) * 1024) + sw128;
    const char* w2c = (const char*)w2 + cd * 128 + (size_t)(wq * 448) * 1024;
#define W2SLOT(s) ((s) < 3 ? 131072u + (s) * 8192u : (s) < 19 ? ((s) - 3) * 8192u : (s) < 22 ? 131072u + ((s) - 19) * 8192u : ((s) - 22) * 8192u)
#define W2DMA(s) dma16(w2c + (s) * 16384 + wh * 8192, w2v, lds0 + W2SLOT(s) + wq * 2048 + wh * 1024)
    const u32 vo16 = (u32)lane * 16u;
    const u32 la = (u32)(wq * 2048 + (4 * th + tq) * 128 + (((2 * tG + (tp_ >> 1)) ^ ((tq >> 1) << 2)) * 16) + 8 * (tp_ & 1));
    bf16x8 AF[28][2];
#define AFRAG(n) do { const u32 a_ = la + W2SLOT(n); AF[n][0] = trfrag(lds + a_, lds + a_ + 1024); AF[n][1] = trfrag(lds + (a_ ^ 64u), lds + (a_ ^ 64u) + 1024); } while (0)
    if (g1on) {
        bf16x8 WA[32];
        {
            const u32 wa = (u32)((8 * th + tq) * 256 + (((4 * wq + 2 * tG + (tp_ >> 1)) ^ (tq << 2)) * 16) + 8 * (tp_ & 1)) + 65536u;
#pragma unroll
            for (int i = 0; i < 24; ++i) { LAS char* a = lds + (wa + (i >= 16 ? 65536u : 0u)) + (i & 15) * 4096; WA[i] = trfrag(a, a + 1024); }
            VMW(0);
            BAR();
#pragma unroll
            for (int i = 24; i < 32; ++i) { LAS char* a = lds + (wa - 32768u) + (i - 24) * 4096; WA[i] = trfrag(a, a + 1024); }
        }
        STAMP(12);
        if (wave < 4) wait_flags<2>(ws, WS_FLAG1 + (u32)tid * 128u, tag);
        BAR();
        STAMP(13);
        const char* hsrc = ws + WS_H1 + (size_t)rt1 * 131072 + wave * 2048;
        const u32 hv_ = (u32)((lane >> 3) * 128) + sw128;
#define HDMA(k) dma16_dev(hsrc + ((k) >> 1) * 16384 + ((k) & 1) * 1024, hv_, lds0 + ((k) >> 1) * 16384 + wave * 2048 + ((k) & 1) * 1024)
#pragma unroll
        for (int k = 0; k < 6; ++k) HDMA(k);
        f32x16 acc0 = {}, acc1 = {};
        u32 lb = (u32)((8 * th + tq) * 128 + (((4 * wh + 2 * tG + (tp_ >> 1)) ^ ((tq >> 1) << 2)) * 16) + 8 * (tp_ & 1));
        constexpr int BD = 4;
        bf16x8 Bf[64];
        u32x2 g0, g1, gc;
        bf16x8 CSF;
        const u32 oc = WS_CS + (u32)((ct * 128 + wq * 32 + (lane & 31)) * 8);
        const u32 og = WS_ST2 + (u32)((rt1 * 128 + wh * 64 + (lane & 31)) * 8);
        const int ks = ct * 8 + wq * 2, kqp = ks / 28;
        const char* hdst = ws + WS_HH + (size_t)(((rt1 * 2 + wh) * 4 + kqp) * 56 + (ks - kqp * 28)) * 1024;
#define G1_STORE(acc, gm, off) do { u32x4 hq[2]; \
            const u32 nm_ = th ? 0u : (((gm).x & 0xffffu) ^ 0x8000u) * 0x10001u; const u32x4 mq_ = {nm_, 0u, 0u, 0u}; \
            acc = __builtin_amdgcn_mfma_f32_32x32x16_bf16(CSF, __builtin_bit_cast(bf16x8, mq_), acc, 0, 0, 0); \
            _Pragma("unroll") for (int k = 0; k < 8; ++k) { \
                const s16x2 zz = {0, 0}; \
                hq[k >> 2][k & 3] = __builtin_bit_cast(u32, __builtin_elementwise_max(__builtin_bit_cast(s16x2, pkbf(acc[2 * k], acc[2 * k + 1])), zz)); } \
            asm volatile("global_store_dwordx4 %0, %1, %3 sc1\n\tglobal_store_dwordx4 %0, %2, %3 offset:1024 sc1\n\ts_nop 1" \
                         :: "v"(vo16), "v"(hq[0]), "v"(hq[1]), "s"(hdst + (off)) : "memory"); } while (0)
        VMW(4);
        BAR();
        STAMP(14);
#pragma unroll
        for (int r = 0; r < BD; ++r) Bf[r] = trfrag(lds + lb + r * 2048, lds + lb + r * 2048 + 512);
#pragma unroll
        for (int c = 0; c < 8; ++c) {
            if (c < 5) VMW(2 + 2 * c); else if (c == 5) VMW(15);
            SBAR();
#pragma unroll
            for (int r = 0; r < 8; ++r) {
                const int gs = 8 * c + r, gn = gs + BD;
                if (gn == 32) lb += 65536u;
                if (gn < 64) Bf[gn < 64 ? gn : 63] = trfrag(lds + lb + (gn & 31) * 2048, lds + lb + (gn & 31) * 2048 + 512);
                if (c < 4) acc0 = __builtin_amdgcn_mfma_f32_32x32x16_bf16(WA[gs & 31], Bf[gs], acc0, 0, 0, 0);
                else acc1 = __builtin_amdgcn_mfma_f32_32x32x16_bf16(WA[gs & 31], Bf[gs], acc1, 0, 0, 0);
                if ((r & 1) == 0) {
                    const int n = 4 * c + (r >> 1);
                    if (n == 19)
                        asm volatile("global_load_dwordx2 %2, %4, %5 sc1\n\tglobal_load_dwordx2 %0, %3, %5 sc1\n\tglobal_load_dwordx2 %1, %3, %5 offset:256 sc1"
                                     : "=&v"(g0), "=&v"(g1), "=&v"(gc) : "v"(og), "v"(oc), "s"(ws) : "memory");
                    if (n < 10) HDMA(6 + n);
                    else if (n < 23) W2DMA(n - 10);
                    else if (n == 24 || n == 25) W2DMA(n - 11);
                    else if (n == 28 || n == 29) W2DMA(n - 13);
                }
                SCHED();
            }
            if (c == 5) {
                asm volatile("s_waitcnt vmcnt(4)" : "+v"(g0), "+v"(g1), "+v"(gc) :: "memory");
#pragma nounroll
                for (int spin = 0; spin < SPIN_CAP; ++spin) {
                    if (__all(g0.y == tag && g1.y == tag && gc.y == tag)) break;
                    __builtin_amdgcn_s_sleep(2);
                    asm volatile("global_load_dwordx2 %0, %3, %5 sc1\n\tglobal_load_dwordx2 %1, %3, %5 offset:256 sc1\n\tglobal_load_dwordx2 %2, %4, %5 sc1\n\ts_waitcnt vmcnt(0)"
                                 : "=&v"(g0), "=&v"(g1), "=&v"(gc) : "v"(og), "v"(oc), "s"(ws) : "memory");
                }
                { const u32x4 cq = {gc.x, 0u, 0u, 0u}; CSF = __builtin_bit_cast(bf16x8, cq); }
                G1_STORE(acc0, g0, 0);
                SCHED();
            }
        }
        STAMP(15);
        G1_STORE(acc1, g1, 28672);
#pragma unroll
        for (int i = 0; i < 9; ++i) AFRAG(i);
        VMW(0);
    } else {
        if (sr < 28) {
            float cs[8] = {};
#pragma unroll
            for (int k = 0; k < 8; ++k) {
                asm volatile("" : "+v"(wv[k]));
#pragma unroll
                for (int e = 0; e < 4; ++e) { const u32 d = wv[k][e]; cs[2 * e] += bflo(d); cs[2 * e + 1] += bfhi(d); }
            }
#pragma unroll
            for (int e = 0; e < 8; ++e) cs[e] = dpp_add<0x128>(cs[e]);
            if ((lane & 8) == 0) {
                LAS char* pp = lds + (wave * 4 + (lane >> 4)) * 256 + (lane & 7) * 32;
                const f32x4 v0 = {cs[0], cs[1], cs[2], cs[3]}, v1 = {cs[4], cs[5], cs[6], cs[7]};
                *(LAS f32x4*)(pp) = v0; *(LAS f32x4*)(pp + 16) = v1;
            }
            BAR();
            if (wave == 7) {
                float S = 0.f;
#pragma unroll
                for (int k = 0; k < 32; ++k) S += *(LAS float*)(lds + k * 256 + lane * 4);
                put_granule(ws, WS_CS + (u32)((sr * 64 + lane) * 8), u32x2{pkbf(S, S - rbf(S)), tag});
            }
        }
        STAMP(12);
        if (wave < 4) wait_flags<2>(ws, WS_FLAG1 + (u32)tid * 128u, tag);
        BAR();
        STAMP(13);
        const char* src = ws + WS_H1 + (size_t)sr * 65536 + wave * 8192;
        u32x4 hs[8];
#pragma unroll
        for (int k = 0; k < 8; ++k) asm volatile("global_load_dwordx4 %0, %1, %2 offset:%3 sc1" : "=v"(hs[k]) : "v"(vo16), "s"(src + (k >> 2) * 4096), "i"((k & 3) * 1024) : "memory");
        STAMP(14);
        float sm[8] = {}, sq[8] = {};
#pragma unroll
        for (int k = 0; k < 8; ++k) {
            asm volatile("s_waitcnt vmcnt(%1)" : "+v"(hs[k]) : "i"(7 - k) : "memory");
#pragma unroll
            for (int e = 0; e < 4; ++e) {
                const u32 d = hs[k][e];
                const float lo = bflo(d), hi = bfhi(d);
                sm[2 * e] += lo; sq[2 * e] = __builtin_fmaf(lo, lo, sq[2 * e]); sm[2 * e + 1] += hi; sq[2 * e + 1] = __builtin_fmaf(hi, hi, sq[2 * e + 1]);
            }
        }
#pragma unroll
        for (int e = 0; e < 8; ++e) { sm[e] = dpp_add<0x128>(sm[e]); sq[e] = dpp_add<0x128>(sq[e]); }
        if ((lane & 8) == 0) {
            LAS char* pp = lds + 114688 + (wave * 4 + (lane >> 4)) * 512 + (lane & 7) * 64;
#pragma unroll
            for (int e = 0; e < 4; ++e) { const f32x4 v = {sm[2 * e], sq[2 * e], sm[2 * e + 1], sq[2 * e + 1]}; *(LAS f32x4*)(pp + e * 16) = v; }
        }
        BAR();
        if (wave == 0) {
            float S = 0.f, Q = 0.f;
#pragma unroll
            for (int k = 0; k < 32; ++k) { const f32x2 v = *(LAS f32x2*)(lds + 114688 + k * 512 + lane * 8); S += v.x; Q += v.y; }
            const float mean = S * (1.f / 512.f), var = fmaxf(Q * (1.f / 512.f) - mean * mean, 0.f);
            put_granule(ws, WS_ST2 + (u32)(((sr >> 1) * 128 + (lane >> 5) * 64 + (sr & 1) * 32 + (lane & 31)) * 8),
                        u32x2{pkbf(mean, __builtin_amdgcn_sqrtf(rbf(rbf(var) + 1e-5f))), tag});
        }
        STAMP(15);
        BAR();
#pragma unroll
        for (int s = 0; s < 19; ++s) W2DMA(s);
        VMW(10);
        BAR();
#pragma unroll
        for (int i = 0; i < 9; ++i) AFRAG(i);
    }
    BAR();
    if (g1on && tid == 0) put_word(ws, WS_FLAG2 + (u32)(rt1 * 14 + ct) * 128u, tag);
    if (bid == 0 && tid == 64) put_word(ws, 0u, tag);
    STAMP(16);
    if (g1on) { W2DMA(17); W2DMA(18); }
#pragma unroll
    for (int s = 19; s < 28; ++s) W2DMA(s);
    dma16_dev(ws + WS_H1 + (size_t)(r64 >> 1) * 131072 + (size_t)(cd * 64 + wq * 16) * 128 + wh * 65536,
              (u32)((lane >> 2) * 128 + (r64 & 1) * 64 + (lane & 3) * 16), lds0 + L_RES + wave * 1024);
    if (wave == 3) wait_flags<4>(ws, WS_FLAG2 + (u32)((r64 >> 1) * 14 + (lane < 14 ? lane : 0)) * 128u, tag);
    SBAR();
    constexpr int PF = 8;
    const char* h2src = ws + WS_HH + (size_t)((r64 * 4 + wq) * 2 + wh) * 28672;
    const u32 ogd = WS_ST2 + (u32)((r64 * 64 + wh * 32 + (lane & 31)) * 8);
    u32x4 hv[28];
    u32x2 gd;
#define HLOAD(i) asm volatile("global_load_dwordx4 %0, %1, %2 offset:%3 sc1" : "=v"(hv[i]) : "v"(vo16), "s"(h2src + ((i) >> 2) * 4096), "i"(((i) & 3) * 1024) : "memory")
    asm volatile("global_load_dwordx2 %0, %1, %2 sc1" : "=v"(gd) : "v"(ogd), "s"(ws) : "memory");
#pragma unroll
    for (int i = 0; i < PF; ++i) HLOAD(i);
    if (wh) { VMW(PF + 1); SBAR(); }
    STAMP(17);
    f32x16 o0 = {}, o1 = {};
#pragma unroll
    for (int i = 0; i < 28; ++i) {
        if (i == 6 || i == 13) { if (wh == 0 && g1on == (i == 13)) SBAR(); }
        if (i == 12 || i == 18) { if (wh) SBAR(); }
        if (i + 3 >= 9 && i + 3 < 28) AFRAG(i + 3 < 28 ? i + 3 : 27);
        asm volatile("s_waitcnt vmcnt(%1)" : "+v"(hv[i]) : "i"(i + PF <= 28 ? PF - 1 : 27 - i) : "memory");
        if (i + PF < 28) HLOAD(i + PF < 28 ? i + PF : 27);
        const bf16x8 Bq = __builtin_bit_cast(bf16x8, hv[i]);
        o0 = __builtin_amdgcn_mfma_f32_32x32x16_bf16(AF[i][0], Bq, o0, 0, 0, 0);
        o1 = __builtin_amdgcn_mfma_f32_32x32x16_bf16(AF[i][1], Bq, o1, 0, 0, 0);
        SCHED();
    }
    STAMP(18);
    const u32 ar = L_RES + (u32)(wh * 4096 + (wq * 16 + 4 * th + tq) * 64 + tG * 32 + tp_ * 8);
    const u32x4 rs = __builtin_bit_cast(u32x4, trfrag(lds + ar, lds + ar + 512));
    asm volatile("" : "+v"(gd));
#pragma nounroll
    for (int spin = 0; spin < SPIN_CAP; ++spin) {
        if (__all(gd.y == tag)) break;
        asm volatile("global_load_dwordx2 %0, %1, %2 sc1\n\ts_waitcnt vmcnt(0)" : "=&v"(gd) : "v"(ogd), "s"(ws) : "memory");
    }
    const float ri = __builtin_amdgcn_rcpf(bfhi(gd.x));
    u16* op = out + (size_t)(r64 * 64 + wh * 32 + (lane & 31)) * 512 + cd * 64 + wq * 16 + th * 8;
    if (wh == 0) BAR();
    {
        LAS char* xp = lds + 49152 + wh * 24576 + lane * 16 - 2048;
        f32x4 s0, s1;
#pragma unroll
        for (int dq = 0; dq < 4; ++dq) {
            const f32x16& o = (dq >> 1) ? o1 : o0;
            const int r = 8 * (dq & 1);
            const f32x4 v0 = {o[r], o[r + 1], o[r + 2], o[r + 3]}, v1 = {o[r + 4], o[r + 5], o[r + 6], o[r + 7]};
            if (dq == wq) { s0 = v0; s1 = v1; }
            else { LAS char* wp = xp + dq * 6144 + (wq ^ dq) * 2048; *(LAS f32x4*)(wp) = v0; *(LAS f32x4*)(wp + 1024) = v1; }
        }
        BAR();
        LAS char* rp = xp + wq * 6144;
#pragma unroll
        for (int s = 1; s < 4; ++s) { s0 += *(LAS f32x4*)(rp + s * 2048); s1 += *(LAS f32x4*)(rp + s * 2048 + 1024); }
        u32 p0 = pkbf(__builtin_fmaf(s0[0], ri, bflo(rs.x)), __builtin_fmaf(s0[1], ri, bfhi(rs.x))), p1 = pkbf(__builtin_fmaf(s0[2], ri, bflo(rs.y)), __builtin_fmaf(s0[3], ri, bfhi(rs.y)));
        u32 p2 = pkbf(__builtin_fmaf(s1[0], ri, bflo(rs.z)), __builtin_fmaf(s1[1], ri, bfhi(rs.z))), p3 = pkbf(__builtin_fmaf(s1[2], ri, bflo(rs.w)), __builtin_fmaf(s1[3], ri, bfhi(rs.w)));
        auto y0 = __builtin_amdgcn_permlane32_swap(p0, p2, false, false);
        auto y1 = __builtin_amdgcn_permlane32_swap(p1, p3, false, false);
        const u32x4 ov = {y0[0], y1[0], y0[1], y1[1]};
        *(u32x4*)op = ov;
    }
    STAMP(19);
    if (wh == 0) SBAR();
}

extern "C" void kernel_launch(void* const* d_in, const int* in_sizes, int n_in, void* d_out, int out_size, void* d_ws, size_t ws_size,
                              hipStream_t stream) {
    (void)hipFuncSetAttribute((const void*)fused_s4d_mlp, hipFuncAttributeMaxDynamicSharedMemorySize, LDS_BYTES);
    fused_s4d_mlp<<<256, 512, LDS_BYTES, stream>>>((const u16*)d_in[0], (const u16*)d_in[1], (const u16*)d_in[2], (const u16*)d_in[3],
                                                    (const u16*)d_in[4], (const u16*)d_in[5], (const u16*)d_in[6], (const u16*)d_in[8],
                                                    (const u16*)d_in[9], (u16*)d_out, (char*)d_ws);
}
```
